```python
import torch

def _softmax(x, axis=-1):
    return torch.softmax(x, dim=axis)

def _gelu(x):
    return torch.nn.functional.gelu(x, approximate='tanh')

def _silu(x):
    return torch.nn.functional.silu(x)

def _sigmoid(x):
    return torch.sigmoid(x)

def _layernorm(x, axis=-1, eps=1e-05):
    m = x.mean(dim=axis, keepdim=True)
    v = x.var(dim=axis, keepdim=True, unbiased=False)
    return (x - m) / torch.sqrt(v + eps)

def _scatter_add(tgt, idx, src, axis=0):
    return tgt.clone().index_add_(axis, idx, src)

def _segment_sum(src, seg, n):
    out = torch.zeros((n,) + src.shape[1:], dtype=src.dtype)
    return out.index_add_(0, seg, src)

def _pad(x, pw):
    flat = tuple((v for pair in reversed(pw) for v in pair))
    return torch.nn.functional.pad(x, flat)
kernel_name = 'gated_mix__memory_aug_recurrent_mem_tr__sliding_block__prompt'

def setup_inputs():

    _w = lambda a: (lambda t: t.to(torch.bfloat16) if t.is_floating_point() else t)(torch.as_tensor(a))

    def _A_setup():
        B, S, Dm = (1, 2048, 576)
        H, Dh = (32, 128)
        M, seg = (8, 32)
        seg = min(seg, S)
        S = S // seg * seg
        return {'x': _w((lambda *s: torch.randn(*s, dtype=torch.bfloat16) * 0.05)(B, S, Dm)), 'mem0': _w((lambda *s: torch.randn(*s, dtype=torch.bfloat16) * 1.6)(B, M, Dm)), 'Wq': _w((lambda *s: torch.randn(*s, dtype=torch.bfloat16) * 0.8)(Dm, H, Dh) * Dm ** (-0.5)), 'Wk': _w((lambda *s: torch.randn(*s, dtype=torch.bfloat16) * 0.8)(Dm, H, Dh) * Dm ** (-0.5)), 'Wv': _w((lambda *s: torch.randn(*s, dtype=torch.bfloat16) * 1.6)(Dm, H, Dh) * Dm ** (-0.5)), 'Wo': _w((lambda *s: torch.randn(*s, dtype=torch.bfloat16) * 1.6)(H, Dh, Dm) * (H * Dh) ** (-0.5)), 'seg_len': seg}
    _A_in = _A_setup()

    def _B_setup():
        B, S, D = (1, 2048, 576)
        return {'x': _w((lambda *s: torch.randn(*s, dtype=torch.bfloat16) * 0.05)(B, S, D)), 'wqkv': _w((lambda *s: torch.randn(*s, dtype=torch.bfloat16) * 1.6)(D, 3 * D)), 'wo': _w((lambda *s: torch.randn(*s, dtype=torch.bfloat16) * 0.05)(D, D))}
    _B_in = _B_setup()
    return {'A_x': _A_in['x'], 'A_mem0': _A_in['mem0'], 'A_Wq': _A_in['Wq'], 'A_Wk': _A_in['Wk'], 'A_Wv': _A_in['Wv'], 'A_Wo': _A_in['Wo'], 'A_seg_len': _A_in['seg_len'], 'B_x': _B_in['x'], 'B_wqkv': _B_in['wqkv'], 'B_wo': _B_in['wo'], 'gate_w': _w((lambda *s: torch.randn(*s, dtype=torch.bfloat16) * 0.05)(576) * 16.0)}

def reference(A_x, A_mem0, A_Wq, A_Wk, A_Wv, A_Wo, A_seg_len, B_x, B_wqkv, B_wo, gate_w):

    def _A_ref(x, mem0, Wq, Wk, Wv, Wo, seg_len):
        B, S, Dm = x.shape
        H, Dh = (Wq.shape[1], Wq.shape[2])
        M = mem0.shape[1]
        scale = Dh ** (-0.5)
        n_seg = S // seg_len
        L = M + seg_len
        idx = torch.arange(L)
        mask = (idx[:, None] < M) | (idx[:, None] >= idx[None, :])
        mem = mem0
        outs = []
        for s in range(n_seg):
            xs = x[:, s * seg_len:(s + 1) * seg_len, :].clone()
            h = torch.concatenate([mem, xs], axis=1)
            q = torch.einsum('btd,dhe->bhte', h, Wq)
            k = torch.einsum('btd,dhe->bhte', h, Wk)
            v = torch.einsum('btd,dhe->bhte', h, Wv)
            scores = torch.einsum('bhqe,bhke->bhqk', q, k) * scale
            scores = torch.where(mask[None, None], scores, -1000000000.0)
            p = _softmax(scores, axis=-1)
            o = torch.einsum('bhqk,bhke->bhqe', p, v)
            o = torch.einsum('bhte,hed->btd', o, Wo)
            mem = o[:, :M, :].clone()
            outs.append(o[:, M:, :])
        return torch.concatenate(outs, axis=1)

    def _B_ref(x, wqkv, wo):
        B, S, D = x.shape
        H = 32
        Dh = D // H
        W = 256
        qkv = (lambda x, *d: x.permute(*d))((x @ wqkv).reshape(B, S, 3, H, Dh), 2, 0, 3, 1, 4)
        q, k, v = (qkv[0], qkv[1], qkv[2])
        scores = torch.einsum('bhqd,bhkd->bhqk', q, k) * Dh ** (-0.5)
        idx = torch.arange(S)
        mask = (idx[:, None] - idx[None, :] >= 0) & (idx[:, None] - idx[None, :] < W)
        scores = torch.where(mask[None, None], scores, -1000000000.0)
        p = _softmax(scores, axis=-1)
        o = torch.einsum('bhqk,bhkd->bhqd', p, v)
        return (lambda x, *d: x.permute(*d))(o, 0, 2, 1, 3).reshape(B, S, D) @ wo
    x = A_x
    g = _sigmoid((x * gate_w).sum(-1, keepdims=True))
    a = _A_ref(x, A_mem0, A_Wq, A_Wk, A_Wv, A_Wo, A_seg_len)
    b = _B_ref(x, B_wqkv, B_wo)
    return g * a + (1 - g) * b
```

```cpp
#include <hip/hip_runtime.h>
#include <hip/hip_bf16.h>
#include <stdint.h>

typedef unsigned short u16;
__device__ __forceinline__ float bf2f(u16 v) { return __uint_as_float(((unsigned)v) << 16); }
__device__ __forceinline__ u16 f2bf(float f) {
    unsigned u = __float_as_uint(f);
    if ((u & 0x7fffffffu) > 0x7f800000u) return (u16)((u >> 16) | 0x40);
    return (u16)((u + 0x7FFFu + ((u >> 16) & 1u)) >> 16);
}
__device__ __forceinline__ float rbf(float f) { return bf2f(f2bf(f)); }

__global__ void gemm_naive(const u16* A, int lda, const u16* B, int ldb, u16* C, int ldc, int M, int N, int K) {
    int n = blockIdx.x * blockDim.x + threadIdx.x;
    int m = blockIdx.y;
    if (n >= N) return;
    float acc = 0.f;
    for (int k = 0; k < K; ++k) acc += bf2f(A[(size_t)m * lda + k]) * bf2f(B[(size_t)k * ldb + n]);
    C[(size_t)m * ldc + n] = f2bf(acc);
}

__global__ void attnA(const u16* qkv_m, const u16* qkv_x, u16* o, int ldo, int r0) {
    int r = r0 + blockIdx.x, h = blockIdx.y, t = threadIdx.x;
    __shared__ float sc[40];
    __shared__ float p[40];
    const float scale = 0.08838834764831845f;
    auto rowptr = [&](int i) -> const u16* { return i < 8 ? qkv_m + (size_t)i * 12288 : qkv_x + (size_t)(i - 8) * 12288; };
    if (t < 40) {
        const u16* q = rowptr(r) + h * 128;
        const u16* k = rowptr(t) + 4096 + h * 128;
        float acc = 0.f;
        for (int e = 0; e < 128; ++e) acc += bf2f(q[e]) * bf2f(k[e]);
        float s = rbf(rbf(acc) * scale);
        bool ok = (r < 8) || (r >= t);
        sc[t] = ok ? s : rbf(-1e9f);
    }
    __syncthreads();
    if (t < 40) {
        float m = -INFINITY;
        for (int j = 0; j < 40; ++j) m = fmaxf(m, sc[j]);
        float sum = 0.f;
        for (int j = 0; j < 40; ++j) sum += __expf(sc[j] - m);
        p[t] = rbf(__expf(sc[t] - m) / sum);
    }
    __syncthreads();
    float acc = 0.f;
    for (int j = 0; j < 40; ++j) acc += p[j] * bf2f(rowptr(j)[8192 + h * 128 + t]);
    o[(size_t)blockIdx.x * ldo + h * 128 + t] = f2bf(acc);
}

__global__ void attnB(const u16* qkv, u16* o) {
    int i = blockIdx.x, h = blockIdx.y, t = threadIdx.x;
    __shared__ float sc[256];
    __shared__ float red[256];
    const float scale = 0.23570226039551584f;
    int j = i - 255 + t;
    float s = -INFINITY;
    if (j >= 0) {
        float acc = 0.f;
        for (int d = 0; d < 18; ++d) acc += bf2f(qkv[(size_t)i * 1728 + h * 18 + d]) * bf2f(qkv[(size_t)j * 1728 + 576 + h * 18 + d]);
        s = rbf(rbf(acc) * scale);
    }
    sc[t] = s; red[t] = s;
    __syncthreads();
    for (int o2 = 128; o2 > 0; o2 >>= 1) { if (t < o2) red[t] = fmaxf(red[t], red[t + o2]); __syncthreads(); }
    float m = red[0]; __syncthreads();
    float e = (j >= 0) ? __expf(s - m) : 0.f;
    red[t] = e; __syncthreads();
    for (int o2 = 128; o2 > 0; o2 >>= 1) { if (t < o2) red[t] += red[t + o2]; __syncthreads(); }
    float sum = red[0]; __syncthreads();
    sc[t] = rbf(e / sum);
    __syncthreads();
    if (t < 18) {
        float acc = 0.f;
        for (int tt = 0; tt < 256; ++tt) { int jj = i - 255 + tt; if (jj >= 0) acc += sc[tt] * bf2f(qkv[(size_t)jj * 1728 + 1152 + h * 18 + t]); }
        o[(size_t)i * 576 + h * 18 + t] = f2bf(acc);
    }
}

__global__ void combine(const u16* x, const u16* gw, const u16* a, const u16* b, u16* out) {
    int i = blockIdx.x, t = threadIdx.x;
    float acc = 0.f;
    for (int d = t; d < 576; d += 64) acc += rbf(bf2f(x[(size_t)i * 576 + d]) * bf2f(gw[d]));
    for (int o = 32; o > 0; o >>= 1) acc += __shfl_xor(acc, o);
    float sg = rbf(acc);
    float g = rbf(1.f / (1.f + __expf(-sg)));
    float omg = rbf(1.f - g);
    for (int d = t; d < 576; d += 64) {
        float v = rbf(g * bf2f(a[(size_t)i * 576 + d])) + rbf(omg * bf2f(b[(size_t)i * 576 + d]));
        out[(size_t)i * 576 + d] = f2bf(v);
    }
}

extern "C" void kernel_launch(void* const* d_in, const int* in_sizes, int n_in,
                              void* d_out, int out_size, void* d_ws, size_t ws_size,
                              hipStream_t stream) {
    const u16* x = (const u16*)d_in[0];
    const u16* mem0 = (const u16*)d_in[1];
    const u16* Wq = (const u16*)d_in[2];
    const u16* Wk = (const u16*)d_in[3];
    const u16* Wv = (const u16*)d_in[4];
    const u16* Wo = (const u16*)d_in[5];
    const u16* wqkv = (const u16*)d_in[8];
    const u16* wo = (const u16*)d_in[9];
    const u16* gw = (const u16*)d_in[10];
    u16* out = (u16*)d_out;
    char* ws = (char*)d_ws;
    u16* qkvx = (u16*)ws; ws += (size_t)2048 * 12288 * 2;
    u16* qkvm = (u16*)ws; ws += (size_t)64 * 8 * 12288 * 2;
    u16* mem = (u16*)ws; ws += (size_t)65 * 8 * 576 * 2 + 1024;
    u16* om = (u16*)ws; ws += (size_t)8 * 4096 * 2;
    u16* ox = (u16*)ws; ws += (size_t)2048 * 4096 * 2;
    u16* a = (u16*)ws; ws += (size_t)2048 * 576 * 2;
    u16* qkvb = (u16*)ws; ws += (size_t)2048 * 1728 * 2;
    u16* ob = (u16*)ws; ws += (size_t)2048 * 576 * 2;
    u16* b = (u16*)ws; ws += (size_t)2048 * 576 * 2;

    const u16* W3[3] = {Wq, Wk, Wv};
    for (int i = 0; i < 3; ++i)
        gemm_naive<<<dim3(4096 / 256, 2048), 256, 0, stream>>>(x, 576, W3[i], 4096, qkvx + i * 4096, 12288, 2048, 4096, 576);
    hipMemcpyAsync(mem, mem0, 8 * 576 * 2, hipMemcpyDeviceToDevice, stream);
    for (int s = 0; s < 64; ++s) {
        u16* ms = mem + (size_t)s * 8 * 576;
        u16* qm = qkvm + (size_t)s * 8 * 12288;
        for (int i = 0; i < 3; ++i)
            gemm_naive<<<dim3(4096 / 64, 8), 64, 0, stream>>>(ms, 576, W3[i], 4096, qm + i * 4096, 12288, 8, 4096, 576);
        attnA<<<dim3(8, 32), 128, 0, stream>>>(qm, qkvx + (size_t)s * 32 * 12288, om, 4096, 0);
        attnA<<<dim3(32, 32), 128, 0, stream>>>(qm, qkvx + (size_t)s * 32 * 12288, ox + (size_t)s * 32 * 4096, 4096, 8);
        gemm_naive<<<dim3(9, 8), 64, 0, stream>>>(om, 4096, Wo, 576, ms + 8 * 576, 576, 8, 576, 4096);
    }
    gemm_naive<<<dim3(9, 2048), 64, 0, stream>>>(ox, 4096, Wo, 576, a, 576, 2048, 576, 4096);
    gemm_naive<<<dim3(27, 2048), 64, 0, stream>>>(x, 576, wqkv, 1728, qkvb, 1728, 2048, 1728, 576);
    attnB<<<dim3(2048, 32), 256, 0, stream>>>(qkvb, ob);
    gemm_naive<<<dim3(9, 2048), 64, 0, stream>>>(ob, 576, wo, 576, b, 576, 2048, 576, 576);
    combine<<<2048, 64, 0, stream>>>(x, gw, a, b, out);
}
```

```cpp
#include <hip/hip_runtime.h>
#include <hip/hip_bf16.h>
#include <stdint.h>

typedef unsigned short u16;
__device__ __forceinline__ float bf2f(u16 v) { return __uint_as_float(((unsigned)v) << 16); }
__device__ __forceinline__ u16 f2bf(float f) {
    unsigned u = __float_as_uint(f);
    if ((u & 0x7fffffffu) > 0x7f800000u) return (u16)((u >> 16) | 0x40);
    return (u16)((u + 0x7FFFu + ((u >> 16) & 1u)) >> 16);
}
__device__ __forceinline__ float rbf(float f) { return bf2f(f2bf(f)); }

__global__ void gemm_naive(const u16* A, int lda, const u16* B, int ldb, u16* C, int ldc, int M, int N, int K) {
    int n = blockIdx.x * blockDim.x + threadIdx.x;
    int m = blockIdx.y;
    if (n >= N) return;
    float acc = 0.f;
    for (int k = 0; k < K; ++k) acc += bf2f(A[(size_t)m * lda + k]) * bf2f(B[(size_t)k * ldb + n]);
    C[(size_t)m * ldc + n] = f2bf(acc);
}

__global__ void attnA(const u16* qkv_m, const u16* qkv_x, u16* o, int ldo, int r0) {
    int r = r0 + blockIdx.x, h = blockIdx.y, t = threadIdx.x;
    __shared__ float sc[40];
    __shared__ float p[40];
    const float scale = 0.08838834764831845f;
    auto rowptr = [&](int i) -> const u16* { return i < 8 ? qkv_m + (size_t)i * 12288 : qkv_x + (size_t)(i - 8) * 12288; };
    if (t < 40) {
        const u16* q = rowptr(r) + h * 128;
        const u16* k = rowptr(t) + 4096 + h * 128;
        float acc = 0.f;
        for (int e = 0; e < 128; ++e) acc += bf2f(q[e]) * bf2f(k[e]);
        float s = acc * scale;
        bool ok = (r < 8) || (r >= t);
        sc[t] = ok ? s : rbf(-1e9f);
    }
    __syncthreads();
    if (t < 40) {
        float m = -INFINITY;
        for (int j = 0; j < 40; ++j) m = fmaxf(m, sc[j]);
        float sum = 0.f;
        for (int j = 0; j < 40; ++j) sum += __expf(sc[j] - m);
        p[t] = __expf(sc[t] - m) / sum;
    }
    __syncthreads();
    float acc = 0.f;
    for (int j = 0; j < 40; ++j) acc += p[j] * bf2f(rowptr(j)[8192 + h * 128 + t]);
    o[(size_t)blockIdx.x * ldo + h * 128 + t] = f2bf(acc);
}

__global__ void attnB(const u16* qkv, u16* o) {
    int i = blockIdx.x, h = blockIdx.y, t = threadIdx.x;
    __shared__ float sc[256];
    __shared__ float red[256];
    const float scale = 0.23570226039551584f;
    int j = i - 255 + t;
    float s = -INFINITY;
    if (j >= 0) {
        float acc = 0.f;
        for (int d = 0; d < 18; ++d) acc += bf2f(qkv[(size_t)i * 1728 + h * 18 + d]) * bf2f(qkv[(size_t)j * 1728 + 576 + h * 18 + d]);
        s = acc * scale;
    }
    sc[t] = s; red[t] = s;
    __syncthreads();
    for (int o2 = 128; o2 > 0; o2 >>= 1) { if (t < o2) red[t] = fmaxf(red[t], red[t + o2]); __syncthreads(); }
    float m = red[0]; __syncthreads();
    float e = (j >= 0) ? __expf(s - m) : 0.f;
    red[t] = e; __syncthreads();
    for (int o2 = 128; o2 > 0; o2 >>= 1) { if (t < o2) red[t] += red[t + o2]; __syncthreads(); }
    float sum = red[0]; __syncthreads();
    sc[t] = e / sum;
    __syncthreads();
    if (t < 18) {
        float acc = 0.f;
        for (int tt = 0; tt < 256; ++tt) { int jj = i - 255 + tt; if (jj >= 0) acc += sc[tt] * bf2f(qkv[(size_t)jj * 1728 + 1152 + h * 18 + t]); }
        o[(size_t)i * 576 + h * 18 + t] = f2bf(acc);
    }
}

__global__ void combine(const u16* x, const u16* gw, const u16* a, const u16* b, u16* out) {
    int i = blockIdx.x, t = threadIdx.x;
    float acc = 0.f;
    for (int d = t; d < 576; d += 64) acc += rbf(bf2f(x[(size_t)i * 576 + d]) * bf2f(gw[d]));
    for (int o = 32; o > 0; o >>= 1) acc += __shfl_xor(acc, o);
    float sg = rbf(acc);
    float g = rbf(1.f / (1.f + __expf(-sg)));
    float omg = rbf(1.f - g);
    for (int d = t; d < 576; d += 64) {
        float v = rbf(g * bf2f(a[(size_t)i * 576 + d])) + rbf(omg * bf2f(b[(size_t)i * 576 + d]));
        out[(size_t)i * 576 + d] = f2bf(v);
    }
}

extern "C" void kernel_launch(void* const* d_in, const int* in_sizes, int n_in,
                              void* d_out, int out_size, void* d_ws, size_t ws_size,
                              hipStream_t stream) {
    const u16* x = (const u16*)d_in[0];
    const u16* mem0 = (const u16*)d_in[1];
    const u16* Wq = (const u16*)d_in[2];
    const u16* Wk = (const u16*)d_in[3];
    const u16* Wv = (const u16*)d_in[4];
    const u16* Wo = (const u16*)d_in[5];
    const u16* wqkv = (const u16*)d_in[8];
    const u16* wo = (const u16*)d_in[9];
    const u16* gw = (const u16*)d_in[10];
    u16* out = (u16*)d_out;
    char* ws = (char*)d_ws;
    u16* qkvx = (u16*)ws; ws += (size_t)2048 * 12288 * 2;
    u16* qkvm = (u16*)ws; ws += (size_t)64 * 8 * 12288 * 2;
    u16* mem = (u16*)ws; ws += (size_t)65 * 8 * 576 * 2 + 1024;
    u16* om = (u16*)ws; ws += (size_t)8 * 4096 * 2;
    u16* ox = (u16*)ws; ws += (size_t)2048 * 4096 * 2;
    u16* a = (u16*)ws; ws += (size_t)2048 * 576 * 2;
    u16* qkvb = (u16*)ws; ws += (size_t)2048 * 1728 * 2;
    u16* ob = (u16*)ws; ws += (size_t)2048 * 576 * 2;
    u16* b = (u16*)ws; ws += (size_t)2048 * 576 * 2;

    const u16* W3[3] = {Wq, Wk, Wv};
    for (int i = 0; i < 3; ++i)
        gemm_naive<<<dim3(4096 / 256, 2048), 256, 0, stream>>>(x, 576, W3[i], 4096, qkvx + i * 4096, 12288, 2048, 4096, 576);
    hipMemcpyAsync(mem, mem0, 8 * 576 * 2, hipMemcpyDeviceToDevice, stream);
    for (int s = 0; s < 64; ++s) {
        u16* ms = mem + (size_t)s * 8 * 576;
        u16* qm = qkvm + (size_t)s * 8 * 12288;
        for (int i = 0; i < 3; ++i)
            gemm_naive<<<dim3(4096 / 64, 8), 64, 0, stream>>>(ms, 576, W3[i], 4096, qm + i * 4096, 12288, 8, 4096, 576);
        attnA<<<dim3(8, 32), 128, 0, stream>>>(qm, qkvx + (size_t)s * 32 * 12288, om, 4096, 0);
        attnA<<<dim3(32, 32), 128, 0, stream>>>(qm, qkvx + (size_t)s * 32 * 12288, ox + (size_t)s * 32 * 4096, 4096, 8);
        gemm_naive<<<dim3(9, 8), 64, 0, stream>>>(om, 4096, Wo, 576, ms + 8 * 576, 576, 8, 576, 4096);
    }
    gemm_naive<<<dim3(9, 2048), 64, 0, stream>>>(ox, 4096, Wo, 576, a, 576, 2048, 576, 4096);
    gemm_naive<<<dim3(27, 2048), 64, 0, stream>>>(x, 576, wqkv, 1728, qkvb, 1728, 2048, 1728, 576);
    attnB<<<dim3(2048, 32), 256, 0, stream>>>(qkvb, ob);
    gemm_naive<<<dim3(9, 2048), 64, 0, stream>>>(ob, 576, wo, 576, b, 576, 2048, 576, 576);
    combine<<<2048, 64, 0, stream>>>(x, gw, a, b, out);
}
```

```cpp
#include <hip/hip_runtime.h>
#include <stdint.h>

typedef unsigned short u16;
typedef __bf16 bf16x8 __attribute__((ext_vector_type(8)));
typedef __bf16 bf16x16 __attribute__((ext_vector_type(16)));
typedef __bf16 bf16x2 __attribute__((ext_vector_type(2)));
typedef float f32x4 __attribute__((ext_vector_type(4)));
typedef float f32x2 __attribute__((ext_vector_type(2)));
typedef unsigned u32x4 __attribute__((ext_vector_type(4)));
typedef unsigned u32x2 __attribute__((ext_vector_type(2)));
typedef unsigned u32x8 __attribute__((ext_vector_type(8)));
typedef short s16x4 __attribute__((ext_vector_type(4)));
typedef __amdgpu_buffer_rsrc_t rsrc_t;
#define LAS __attribute__((address_space(3)))
#define DI __device__ __forceinline__
#define SCHED() __builtin_amdgcn_sched_barrier(0)

DI float bf2f(u16 v) { return __uint_as_float(((unsigned)v) << 16); }
DI unsigned pk2(float a, float b) { f32x2 f = {a, b}; bf16x2 h = __builtin_convertvector(f, bf16x2); return __builtin_bit_cast(unsigned, h); }
DI float rbf(float f) { return __uint_as_float(pk2(f, 0.f) << 16); }
DI f32x4 mfma(u32x4 a, u32x4 b, f32x4 c) { return __builtin_amdgcn_mfma_f32_16x16x32_bf16(__builtin_bit_cast(bf16x8, a), __builtin_bit_cast(bf16x8, b), c, 0, 0, 0); }
DI f32x4 smfmac(u32x4 a, u32x4 b0, u32x4 b1, f32x4 c, int idx) {
    u32x8 b = __builtin_shufflevector(b0, b1, 0, 1, 2, 3, 4, 5, 6, 7);
    return __builtin_amdgcn_smfmac_f32_16x16x64_bf16(__builtin_bit_cast(bf16x8, a), __builtin_bit_cast(bf16x16, b), c, idx, 0, 0);
}
DI u32x2 trread(const void* lds_ptr) {
    s16x4 v = __builtin_amdgcn_ds_read_tr16_b64_v4i16((LAS s16x4*)(lds_ptr));
    return __builtin_bit_cast(u32x2, v);
}
DI int tid_() { int t = threadIdx.x; asm volatile("" : "+v"(t)); return t; }
DI float max2(float a, float b) { float r; asm("v_max_f32 %0, %1, %2" : "=v"(r) : "v"(a), "v"(b)); return r; }
DI float max3(float a, float b, float c) { float r; asm("v_max3_f32 %0, %1, %2, %3" : "=v"(r) : "v"(a), "v"(b), "v"(c)); return r; }

constexpr int NSEG = 64;
constexpr unsigned MB2 = 2u << 20;
constexpr unsigned OFF_HAND = 0;
constexpr int NPAGE = 4;
constexpr unsigned OFF_CTL = NPAGE * MB2;
constexpr unsigned OFF_G = OFF_CTL + 196608;
constexpr unsigned OFF_XF = OFF_CTL + 262144;
constexpr unsigned SZ_XF = 2048u * 576 * 2;
constexpr unsigned OFF_WVOF = OFF_XF + SZ_XF;
constexpr unsigned SZ_WVO = 72u * 16 * 18 * 1024;
constexpr unsigned OFF_TF = OFF_WVOF + SZ_WVO;
constexpr unsigned T_SLOT = 36u * 32 * 256;
constexpr unsigned OFF_QF = OFF_TF + 64u * T_SLOT;
constexpr unsigned SZ_QF = 64u * 32 * 8192;
constexpr unsigned OFF_KF = OFF_QF + SZ_QF;
constexpr unsigned OFF_UF = OFF_KF + SZ_QF;
constexpr unsigned SZ_UF = 64u * 72 * 16384;
constexpr unsigned OFF_PT = OFF_UF + SZ_UF;
constexpr unsigned SZ_PT = 64u * 32 * 2560;
constexpr unsigned OFF_QB = OFF_PT + SZ_PT;
constexpr unsigned SZ_QB = 32u * 2048 * 64;
constexpr unsigned OFF_KB = OFF_QB + SZ_QB;
constexpr unsigned OFF_VB = OFF_KB + SZ_QB;
constexpr unsigned SZ_VB = 32u * 64 * 2048;
constexpr unsigned OFF_OB = OFF_VB + SZ_VB;
constexpr unsigned SZ_OB = 2048u * 576 * 2;
constexpr unsigned OFF_BB = OFF_OB + SZ_OB;
constexpr unsigned WS_END = OFF_BB + SZ_OB;
static_assert((size_t)WS_END + 2 * MB2 <= (256u << 20), "ws too big");
constexpr unsigned C_SEL = OFF_CTL;
constexpr unsigned C_OFF = OFF_CTL + 256;
constexpr unsigned C_K = OFF_CTL + 1024;
constexpr unsigned C_U = C_K + 32 * 128;
constexpr unsigned C_BG = C_U + 32 * 128;
constexpr unsigned C_BA = C_BG + 8 * 128;
constexpr unsigned C_BB = C_BA + 8 * 128;
constexpr unsigned C_P = C_BB + 32 * 128;
constexpr unsigned C_F = C_P + 128 * 128;
constexpr unsigned C_F2 = C_F + 4 * 128;
constexpr unsigned C_TL = C_F2 + 4 * 128;
constexpr unsigned C_END = C_TL + 128;
static_assert(C_END <= OFF_G, "ctl");
constexpr int PF = 3;

DI int perm16(int d16) { return ((d16 >> 1) & 1) * 8 + (d16 >> 2) * 2 + (d16 & 1); }

struct Ptrs {
    const u16* x; const u16* mem0; const u16* Wq; const u16* Wk; const u16* Wv; const u16* Wo;
    const u16* bw; const u16* bo; const u16* gw; u16* out; char* ws;
};

template <typename T> DI T* uni(T* q) {
    unsigned long long v = (unsigned long long)q;
    unsigned lo = __builtin_amdgcn_readfirstlane((unsigned)v), hi = __builtin_amdgcn_readfirstlane((unsigned)(v >> 32));
    return (T*)(__attribute__((address_space(1))) T*)(((unsigned long long)hi << 32) | lo);
}
DI rsrc_t mk_rsrc(char* ws) { return __builtin_amdgcn_make_buffer_rsrc((void*)ws, 0, 0x7ffffff0, 0x00020000); }
DI u32x4 lds16(rsrc_t r, unsigned off) { return __builtin_amdgcn_raw_buffer_load_b128(r, off, 0, 16); }
DI u32x4 ldp16(rsrc_t r, unsigned off) { return __builtin_amdgcn_raw_buffer_load_b128(r, off, 0, 0); }
DI void sts16(rsrc_t r, unsigned off, u32x4 v) { __builtin_amdgcn_raw_buffer_store_b128(v, r, off, 0, 16); }
DI void sts8(rsrc_t r, unsigned off, u32x2 v) { __builtin_amdgcn_raw_buffer_store_b64(v, r, off, 0, 16); }
DI void sts4(rsrc_t r, unsigned off, unsigned v) { __builtin_amdgcn_raw_buffer_store_b32(v, r, off, 0, 16); }
DI void wait_vm0() { asm volatile("s_waitcnt vmcnt(0)" ::: "memory"); }
DI void bar() { asm volatile("s_waitcnt lgkmcnt(0)" ::: "memory"); __builtin_amdgcn_s_barrier(); asm volatile("" ::: "memory"); }
DI void cnt_add(char* ws, unsigned off) { __hip_atomic_fetch_add((unsigned*)(ws + off), 1u, __ATOMIC_RELAXED, __HIP_MEMORY_SCOPE_AGENT); }
constexpr int SPIN_CAP = 1 << 17;
DI void cnt_wait(char* ws, unsigned off, unsigned target) {
    for (int spins = 0; spins < SPIN_CAP; ++spins) {
        unsigned v = __hip_atomic_load((unsigned*)(ws + off), __ATOMIC_RELAXED, __HIP_MEMORY_SCOPE_AGENT);
        if ((unsigned)__builtin_amdgcn_readfirstlane(v) >= target) break;
        __builtin_amdgcn_s_sleep(4);
    }
    asm volatile("" ::: "memory");
}
DI void wg_publish(char* ws, unsigned off) {
    wait_vm0();
    bar();
    if (threadIdx.x == 0) cnt_add(ws, off);
}

DI float xmax4(float x) {
    auto a = __builtin_amdgcn_permlane16_swap(__float_as_uint(x), __float_as_uint(x), false, false);
    x = max2(__uint_as_float(a[0]), __uint_as_float(a[1]));
    auto b = __builtin_amdgcn_permlane32_swap(__float_as_uint(x), __float_as_uint(x), false, false);
    return max2(__uint_as_float(b[0]), __uint_as_float(b[1]));
}
DI float xsum4(float x) {
    auto a = __builtin_amdgcn_permlane16_swap(__float_as_uint(x), __float_as_uint(x), false, false);
    x = __uint_as_float(a[0]) + __uint_as_float(a[1]);
    auto b = __builtin_amdgcn_permlane32_swap(__float_as_uint(x), __float_as_uint(x), false, false);
    return __uint_as_float(b[0]) + __uint_as_float(b[1]);
}

constexpr unsigned MEM_SLOT = 32768;
constexpr unsigned P_SLOT = 49152;
DI unsigned amap(unsigned o, unsigned sel) { return ((o >> 13) << 14) + (sel << 13) + (o & 8191u); }
DI unsigned mem_slot(int copy, int s) { return OFF_HAND + (unsigned)copy * MB2 + (unsigned)s * MEM_SLOT; }
DI unsigned p_slot(int s) { return OFF_HAND + (unsigned)(2 + (s >> 5)) * MB2 + (unsigned)(s & 31) * P_SLOT; }
DI unsigned load_selmask(const char* ws) { const int l = threadIdx.x & 63; unsigned v = l < NPAGE ? ((const unsigned*)(ws + C_SEL))[l] : 0u; return (unsigned)__builtin_amdgcn_ballot_w64(v != 0u); }
DI unsigned nos(unsigned v) { return min(v, 0xFFFFFFFEu); }
DI unsigned long long clk() { unsigned long long t; asm volatile("s_memtime %0\n\ts_waitcnt lgkmcnt(0)" : "=s"(t) :: "memory"); return t; }
DI unsigned xcc_id() { return (unsigned)__builtin_amdgcn_s_getreg(20 | (3 << 11)) & 0xfu; }

constexpr int NB_INIT = 288;
#define SENT ((u32x4){0xFFFFFFFFu, 0xFFFFFFFFu, 0xFFFFFFFFu, 0xFFFFFFFFu})
DI void init_block(const Ptrs& p, int b) {
    const int tid = b * 256 + threadIdx.x, nth = NB_INIT * 256;
    {
        u32x4 v[2];
#pragma unroll
        for (int k = 0; k < 2; ++k) {
            const int c = tid + k * nth, l = c & 63, f = c >> 6, T = f / 72, r = f % 72, tt = r / 18, ks = r % 18;
            v[k] = *(const u32x4*)(p.x + (size_t)(T * 64 + tt * 16 + (l & 15)) * 576 + 32 * ks + 8 * (l >> 4));
        }
#pragma unroll
        for (int k = 0; k < 2; ++k) *(u32x4*)(p.ws + OFF_XF + (unsigned)(tid + k * nth) * 16) = v[k];
    }
    for (int i = tid; i < 2 * 2 * (PF + 1) * 576; i += nth) {
        const int c = i % 576, r = i / 576, s = r % (PF + 1), copy = (r / (PF + 1)) & 1, sel = r / (2 * (PF + 1));
        u32x4 v = SENT;
        if (s == 0) {
            const int j = c >> 3, row = c & 7, G = j >> 1, ph = j & 1;
            const unsigned* m = (const unsigned*)(p.mem0 + row * 576 + 16 * G);
            v = (u32x4){nos(m[ph]), nos(m[2 + ph]), nos(m[4 + ph]), nos(m[6 + ph])};
        }
        *(u32x4*)(p.ws + mem_slot(copy, s) + amap(c * 16, sel)) = v;
    }
    for (int i = tid; i < 2 * PF * 1280; i += nth) {
        const int c = i % 1280, r = i / 1280, s = r % PF, sel = r / PF;
        *(u32x4*)(p.ws + p_slot(s) + amap(c * 16, sel)) = SENT;
    }
    for (int i = tid; i < (int)((C_END - C_OFF) / 16); i += nth) *(u32x4*)(p.ws + C_OFF + i * 16) = (u32x4){0, 0, 0, 0};
    const int wv = tid >> 6, l = threadIdx.x & 63, nw = nth >> 6;
    for (int t = wv; t < 2048; t += nw) {
        float acc = 0.f;
#pragma unroll
        for (int i = 0; i < 9; ++i) acc += rbf(bf2f(p.x[(size_t)t * 576 + l + 64 * i]) * bf2f(p.gw[l + 64 * i]));
#pragma unroll
        for (int o = 32; o > 0; o >>= 1) acc += __shfl_xor(acc, o);
        const float sg = rbf(acc);
        const float g = rbf(1.f / (1.f + __expf(-sg)));
        if (l == 0) ((float*)(p.ws + OFF_G))[t] = g;
    }
}
DI void calib_block(const Ptrs& p) {
    const int pg = blockIdx.x * 4 + (threadIdx.x >> 6), l = threadIdx.x & 63;
    if (pg >= NPAGE) return;
    const unsigned iod = xcc_id() >> 2;
    unsigned best[2] = {1u << 30, 1u << 30};
    const unsigned base = OFF_HAND + pg * MB2 + (pg < 2 ? 63u * MEM_SLOT + 16384u + 4096u : 32u * P_SLOT + 4096u);
    for (int it = 0; it < 4; ++it) {
#pragma unroll
        for (int c = 0; c < 2; ++c) {
            char* a = p.ws + base + c * 8192 + (l & 7) * 16;
            u32x4 v = {1, 2, 3, 4}, r;
            unsigned long long t0 = clk();
            asm volatile("global_store_dwordx4 %0, %1, off sc1\n\ts_waitcnt vmcnt(0)" :: "v"(a), "v"(v) : "memory");
            unsigned long long t1 = clk();
            asm volatile("global_load_dwordx4 %0, %1, off sc1\n\ts_waitcnt vmcnt(0)" : "=&v"(r) : "v"(a) : "memory");
            unsigned long long t2 = clk();
            best[c] = min(best[c], min((unsigned)(t2 - t1), (unsigned)(t1 - t0)));
        }
    }
    unsigned near = best[0] <= best[1] ? 0u : 1u;
    if (l == 0) ((unsigned*)(p.ws + C_SEL))[pg] = near ^ iod;
}
__global__ void __launch_bounds__(256) k_init(Ptrs p) {
    if (blockIdx.x < 1) calib_block(p); else init_block(p, blockIdx.x - 1);
}

#define RT(kind, id, s)
#define RTW(kind, id, s)
#define CT(kind, id, s)
constexpr int XL_BYTES = 8 * 18 * 1024;
constexpr int XBUF = 4 * 18 * 1024;

DI void dma16(const void* sbase, unsigned voff, unsigned ldsa) {
    unsigned keep;
    asm volatile("s_mov_b32 %0, m0\n\ts_mov_b32 m0, %3\n\ts_nop 0\n\tglobal_load_lds_dwordx4 %1, %2\n\ts_mov_b32 m0, %0" : "=&s"(keep) : "v"(voff), "s"(sbase), "s"(ldsa) : "memory");
}
DI unsigned lds_addr(const char* lds) { return __builtin_amdgcn_readfirstlane((unsigned)(size_t)(LAS const char*)lds); }
#define WAITV(n) asm volatile("s_waitcnt vmcnt(" #n ")" ::: "memory")

DI unsigned wblk_lane_off(int N, int w, int l, bool half) {
    const int r = l >> 4, ch0 = (l & 15) ^ ((r << 2) | (w & 3));
    const int ch = half ? (ch0 & 7) : ch0;
    return (unsigned)((4 * w + r) * N + ch * 8) * 2u;
}
DI u32x4 wblk_frag(const char* blk, int c, int l) {
    const int g = l >> 4, q = (l & 15) >> 2, pp = l & 3;
    u32x2 v[2];
#pragma unroll
    for (int t = 0; t < 2; ++t) {
        const int row = 8 * g + 4 * t + q, ch = 2 * c + (pp >> 1);
        v[t] = trread(blk + 256 * row + 16 * (ch ^ (((row & 3) << 2) | ((row >> 2) & 3))) + 8 * (pp & 1));
    }
    return (u32x4){v[0].x, v[0].y, v[1].x, v[1].y};
}
DI void load_w2(const u16* src0, const u16* src1, int N, bool half1, char* lds, int w, int l, u32x4 (&wa)[18], u32x4 (&wb)[18]) {
    const unsigned la = lds_addr(lds);
    const unsigned o0 = wblk_lane_off(N, w, l, false), o1 = wblk_lane_off(N, w, l, half1);
#pragma unroll
    for (int c = 0; c < 2; ++c) {
#pragma unroll
        for (int k = 0; k < 9; ++k) {
            dma16(src0 + (size_t)(32 * (9 * c + k)) * N, o0, la + (2 * k) * 8192 + w * 1024);
            dma16(src1 + (size_t)(32 * (9 * c + k)) * N, o1, la + (2 * k + 1) * 8192 + w * 1024);
        }
        WAITV(0);
        bar();
#pragma unroll
        for (int k = 0; k < 9; ++k) {
            wa[9 * c + k] = wblk_frag(lds + (2 * k) * 8192, w, l);
            wb[9 * c + k] = wblk_frag(lds + (2 * k + 1) * 8192, half1 ? (w & 3) : w, l);
        }
        bar();
    }
}
DI void dma_x(const char* ws, int T, unsigned la, int w, int l) {
#pragma unroll
    for (int i = 0; i < 9; ++i) dma16(ws + OFF_XF + (size_t)(T * 72 + w * 9 + i) * 1024, (unsigned)l * 16u, la + (w * 9 + i) * 1024);
}
template <int NW, bool VPART>
DI void tile_gemm(const char* xb, const u32x4 (&wa)[18], const u32x4 (&wb)[18], f32x4 (&acc)[2][4]) {
    u32x4 xf[2][4];
#pragma unroll
    for (int t = 0; t < 4; ++t) { xf[0][t] = *(const u32x4*)(xb + (t * 18) * 1024); acc[0][t] = (f32x4){0, 0, 0, 0}; acc[1][t] = (f32x4){0, 0, 0, 0}; }
#pragma unroll
    for (int ks = 0; ks < 18; ++ks) {
        if (ks + 1 < 18) {
#pragma unroll
            for (int t = 0; t < 4; ++t) xf[(ks + 1) & 1][t] = *(const u32x4*)(xb + (t * 18 + ks + 1) * 1024);
        }
        SCHED();
#pragma unroll
        for (int t = 0; t < 4; ++t) {
            acc[0][t] = VPART ? mfma(xf[ks & 1][t], wa[ks], acc[0][t]) : mfma(wa[ks], xf[ks & 1][t], acc[0][t]);
            if (NW == 2) acc[1][t] = VPART ? mfma(xf[ks & 1][t], wb[ks], acc[1][t]) : mfma(wb[ks], xf[ks & 1][t], acc[1][t]);
        }
        SCHED();
    }
}
DI u32x4 pair16(const f32x4& a, const f32x4& b) {
    auto x0 = __builtin_amdgcn_permlane16_swap(pk2(a[0], a[1]), pk2(b[0], b[1]), false, false);
    auto x1 = __builtin_amdgcn_permlane16_swap(pk2(a[2], a[3]), pk2(b[2], b[3]), false, false);
    return (u32x4){x0[0], x1[0], x0[1], x1[1]};
}
DI u32x4 pack8(const f32x4& a, const f32x4& b) { return (u32x4){pk2(a[0], a[1]), pk2(a[2], a[3]), pk2(b[0], b[1]), pk2(b[2], b[3])}; }

DI void worker_qk(const Ptrs& p, rsrc_t rs, char* lds, int h, int par) {
    const int w = __builtin_amdgcn_readfirstlane(threadIdx.x >> 6), l = tid_() & 63;
    const int n16 = l & 15, kq = l >> 4;
    u32x4 wa[18], wb[18];
    load_w2(p.Wq + h * 128, p.Wk + h * 128, 4096, false, lds, w, l, wa, wb);
    const unsigned la = lds_addr(lds);
    dma_x(p.ws, par, la, w, l);
    const int ks4 = w >> 1, kqq = 2 * (w & 1) + (kq >> 1), half = kq & 1;
    const unsigned obase = ((unsigned)h * 8 + half * 4 + ks4) * 1024 + (n16 + 16 * kqq) * 16;
#pragma unroll 1
    for (int n = 0; n < 16; ++n) {
        const int T = 2 * n + par;
        RT(8, blockIdx.x, n); CT(8, blockIdx.x, 32 + n);
        if (n < 2) WAITV(0); else WAITV(4);
        bar();
        if (n >= 1 && threadIdx.x == 0) { if (n < 2) cnt_add(p.ws, C_K + (T - 2) * 128); else if (n > 2) cnt_add(p.ws, C_K + (T - 4) * 128); }
        if (n + 1 < 16) dma_x(p.ws, T + 2, la + ((n + 1) & 1) * XBUF, w, l);
        f32x4 acc[2][4];
        tile_gemm<2, false>(lds + (n & 1) * XBUF + l * 16, wa, wb, acc);
#pragma unroll
        for (int sg = 0; sg < 2; ++sg) {
            const unsigned off = obase + (unsigned)(2 * T + sg) * 32 * 8192;
            sts16(rs, OFF_QF + off, pair16(acc[0][2 * sg], acc[0][2 * sg + 1]));
            sts16(rs, OFF_KF + off, pair16(acc[1][2 * sg], acc[1][2 * sg + 1]));
        }
    }
    WAITV(0);
    bar();
    if (threadIdx.x < 2) cnt_add(p.ws, C_K + (28 + 2 * threadIdx.x + par) * 128);
}

DI void load_wvo(const Ptrs& p, char* lds, int h, int w, int l, u32x4 (&wv)[18], u32x4 (&wo)[5][4]) {
    const unsigned la = lds_addr(lds);
    const u16* s0 = p.Wv + h * 128; const unsigned o0 = wblk_lane_off(4096, w, l, false);
#pragma unroll
    for (int k = 0; k < 18; ++k) dma16(s0 + (size_t)(32 * k) * 4096, o0, la + k * 8192 + w * 1024);
    WAITV(0); bar();
#pragma unroll
    for (int k = 0; k < 18; ++k) wv[k] = wblk_frag(lds + k * 8192, w, l);
    bar();
    const u16* s1 = p.Wo + (size_t)h * 128 * 576; const unsigned o1 = wblk_lane_off(576, w, l, false);
    const u16* s2 = p.Wo + (size_t)h * 128 * 576 + 512; const unsigned o2 = wblk_lane_off(576, w, l, true);
#pragma unroll
    for (int cb = 0; cb < 4; ++cb)
#pragma unroll
        for (int k = 0; k < 4; ++k) dma16(s1 + (size_t)(32 * k) * 576 + cb * 128, o1, la + (cb * 4 + k) * 8192 + w * 1024);
#pragma unroll
    for (int k = 0; k < 2; ++k) dma16(s2 + (size_t)(32 * k) * 576, o2, la + (16 + k) * 8192 + w * 1024);
    WAITV(0); bar();
#pragma unroll
    for (int cb = 0; cb < 4; ++cb)
#pragma unroll
        for (int k = 0; k < 4; ++k) wo[cb][k] = wblk_frag(lds + (cb * 4 + k) * 8192, w, l);
#pragma unroll
    for (int k = 0; k < 2; ++k) wo[4][k] = wblk_frag(lds + (16 + k) * 8192, w & 3, l);
    bar();
#pragma unroll
    for (int k = 2; k < 4; ++k) dma16(s2 + (size_t)(32 * k) * 576, o2, la + (k - 2) * 8192 + w * 1024);
    WAITV(0); bar();
#pragma unroll
    for (int k = 2; k < 4; ++k) wo[4][k] = wblk_frag(lds + (k - 2) * 8192, w & 3, l);
    bar();
}

DI void worker_vu(const Ptrs& p, rsrc_t rs, char* lds, int h, int par) {
    const int w = __builtin_amdgcn_readfirstlane(threadIdx.x >> 6), l = tid_() & 63;
    const int n16 = l & 15, kq = l >> 4;
    const unsigned la = lds_addr(lds);
    u32x4 wv[18], wo[5][4];
    load_wvo(p, lds, h, w, l, wv, wo);
    dma_x(p.ws, par, la, w, l);
    dma_x(p.ws, 2 + par, la + XBUF, w, l);
    const int ks4 = w >> 1, kqq = 2 * (w & 1) + (kq >> 1), half = kq & 1;
    const unsigned ub = OFF_UF + ((unsigned)h * 36 + w) * 1024 + (perm16(n16) + 16 * kq) * 16;
#pragma unroll 1
    for (int n = 0; n < 16; ++n) {
        const int T = 2 * n + par;
        char* xb = lds + (n & 1) * XBUF;
        RT(8, blockIdx.x, n); CT(8, blockIdx.x, 32 + n);
        if (n == 0) WAITV(9); else if (n == 1) WAITV(0); else WAITV(17);
        bar();
        if (n >= 1 && threadIdx.x == 0) { if (n < 2) cnt_add(p.ws, C_U + (T - 2) * 128); else if (n > 2) cnt_add(p.ws, C_U + (T - 4) * 128); }
        f32x4 acc[2][4];
        tile_gemm<1, false>(xb + l * 16, wv, wv, acc);
        bar();
#pragma unroll
        for (int t = 0; t < 4; ++t)
            *(u32x2*)(xb + (t * 4 + ks4) * 1024 + (n16 + 16 * kqq) * 16 + half * 8) = (u32x2){pk2(acc[0][t][0], acc[0][t][1]), pk2(acc[0][t][2], acc[0][t][3])};
        bar();
        u32x4 vf[4][4];
#pragma unroll
        for (int t = 0; t < 4; ++t)
#pragma unroll
            for (int ks = 0; ks < 4; ++ks) vf[t][ks] = *(const u32x4*)(xb + (t * 4 + ks) * 1024 + l * 16);
        bar();
        if (n + 2 < 16) dma_x(p.ws, T + 4, la + (n & 1) * XBUF, w, l);
        else {
        }
#pragma unroll
        for (int i = 0; i < 5; ++i) {
            if (i < 4 || w < 4) {
                f32x4 u[4];
#pragma unroll
                for (int t = 0; t < 4; ++t) u[t] = (f32x4){0, 0, 0, 0};
#pragma unroll
                for (int ks = 0; ks < 4; ++ks)
#pragma unroll
                    for (int t = 0; t < 4; ++t) u[t] = mfma(vf[t][ks], wo[i][ks], u[t]);
#pragma unroll
                for (int sg = 0; sg < 2; ++sg) sts16(rs, ub + ((unsigned)(2 * T + sg) * 32 * 36 + 8 * i) * 1024, pack8(u[2 * sg], u[2 * sg + 1]));
            }
        }
        if (n == 14) { WAITV(8); }
    }
    WAITV(0);
    bar();
    if (threadIdx.x < 2) cnt_add(p.ws, C_U + (28 + 2 * threadIdx.x + par) * 128);
}

template <bool VPART>
DI void bgemm_body(const Ptrs& p, rsrc_t rs, char* lds, int cg, int tg) {
    const int w = __builtin_amdgcn_readfirstlane(threadIdx.x >> 6), l = tid_() & 63;
    const int n16 = l & 15, kq = l >> 4;
    u32x4 wa[18], wb[18];
    load_w2(p.bw + cg * 192, p.bw + cg * 192 + 128, 1728, true, lds, w, l, wa, wb);
    const unsigned la = lds_addr(lds);
    dma_x(p.ws, tg * 4, la, w, l);
#pragma unroll 1
    for (int n = 0; n < 4; ++n) {
        const int T = tg * 4 + n;
        WAITV(0);
        bar();
        if (n + 1 < 4) dma_x(p.ws, T + 1, la + ((n + 1) & 1) * XBUF, w, l);
        f32x4 acc[2][4];
        if (w < 4) tile_gemm<2, VPART>(lds + (n & 1) * XBUF + l * 16, wa, wb, acc);
        else tile_gemm<1, VPART>(lds + (n & 1) * XBUF + l * 16, wa, wb, acc);
#pragma unroll
        for (int i = 0; i < 2; ++i) {
            if (i == 0 || w < 4) {
                const int nt = cg * 12 + 8 * i + w;
                if (!VPART) {
#pragma unroll
                    for (int pr = 0; pr < 2; ++pr) {
                        const int n_ = 16 * nt + 4 * kq + 2 * pr;
                        const int part = n_ >= 576, m = n_ - part * 576, h = m / 18, d = m - h * 18;
                        const unsigned base = (part ? OFF_KB : OFF_QB) + ((unsigned)h * 2048 + T * 64 + n16) * 64 + d * 2;
#pragma unroll
                        for (int t = 0; t < 4; ++t) sts4(rs, base + t * 1024, pk2(acc[i][t][2 * pr], acc[i][t][2 * pr + 1]));
                    }
                } else {
                    const int m = 16 * nt + n16 - 1152, h = m / 18, d = m - h * 18;
#pragma unroll
                    for (int b = 0; b < 2; ++b)
                        sts16(rs, OFF_VB + (((unsigned)h * 64 + T * 2 + b) * 2 + (d >> 4)) * 1024 + ((d & 15) + 16 * kq) * 16, pack8(acc[i][2 * b], acc[i][2 * b + 1]));
                }
            }
        }
    }
    wg_publish(p.ws, C_BG + tg * 128);
}

DI u32x4 maskd(u32x4 v, int kq) {
    if (kq >= 2) { v.y = 0; v.z = 0; v.w = 0; }
    if (kq == 3) v.x = 0;
    return v;
}
DI void task_battn(const Ptrs& p, rsrc_t rs, char* lds, int qg, int h) {
    const int w = __builtin_amdgcn_readfirstlane(threadIdx.x >> 6), l = tid_() & 63;
    const int n16 = l & 15, kq = l >> 4;
    if (qg) cnt_wait(p.ws, C_BG + (qg - 1) * 128, 9);
    cnt_wait(p.ws, C_BG + qg * 128, 9);
    const int kqc = kq < 3 ? kq : 2;
    const int b0 = qg * 8 + w;
    {
        u32x4 kv[2][4];
#pragma unroll
        for (int e = 0; e < 2; ++e) {
            int kb = 8 * qg - 8 + 2 * w + e; if (kb < 0) kb = 0;
#pragma unroll
            for (int kt = 0; kt < 2; ++kt) kv[e][kt] = lds16(rs, OFF_KB + ((unsigned)h * 2048 + 32 * kb + 16 * kt + n16) * 64 + kqc * 16);
            kv[e][2] = lds16(rs, OFF_VB + (((unsigned)h * 64 + kb) * 2) * 1024 + l * 16);
            kv[e][3] = lds16(rs, OFF_VB + (((unsigned)h * 64 + kb) * 2 + 1) * 1024 + ((n16 & 1) + 16 * kq) * 16);
        }
#pragma unroll
        for (int e = 0; e < 2; ++e) {
            *(u32x4*)(lds + ((2 * w + e) * 4 + 0) * 1024 + l * 16) = maskd(kv[e][0], kq);
            *(u32x4*)(lds + ((2 * w + e) * 4 + 1) * 1024 + l * 16) = maskd(kv[e][1], kq);
            *(u32x4*)(lds + ((2 * w + e) * 4 + 2) * 1024 + l * 16) = kv[e][2];
            *(u32x4*)(lds + ((2 * w + e) * 4 + 3) * 1024 + l * 16) = kv[e][3];
        }
    }
    u32x4 qf[2];
#pragma unroll
    for (int qt = 0; qt < 2; ++qt) qf[qt] = maskd(lds16(rs, OFF_QB + ((unsigned)h * 2048 + 32 * b0 + 16 * qt + n16) * 64 + kqc * 16), kq);
    bar();
    const char* kvb = lds + w * 4096 + l * 16;
    f32x4 s[2][9][2];
    const float scale = 0.23570226039551584f;
#pragma unroll
    for (int x = 0; x < 9; ++x) {
        const bool valid = b0 - 8 + x >= 0;
#pragma unroll
        for (int kt = 0; kt < 2; ++kt) {
            u32x4 kf = *(const u32x4*)(kvb + (x * 4 + kt) * 1024);
#pragma unroll
            for (int qt = 0; qt < 2; ++qt) {
                f32x4 a = mfma(kf, qf[qt], (f32x4){0, 0, 0, 0});
#pragma unroll
                for (int r = 0; r < 4; ++r) {
                    float v = rbf(rbf(a[r]) * scale);
                    const int diff = 32 * (8 - x) + 16 * (qt - kt) + n16 - 4 * kq - r;
                    bool ok = valid;
                    if (x == 0) ok = ok && (diff < 256);
                    if (x == 8) ok = ok && (diff >= 0);
                    a[r] = ok ? v : -INFINITY;
                }
                s[qt][x][kt] = a;
            }
        }
    }
    u32x4 pf[2][9];
#pragma unroll
    for (int qt = 0; qt < 2; ++qt) {
        float m = -INFINITY;
#pragma unroll
        for (int x = 0; x < 9; ++x)
#pragma unroll
            for (int kt = 0; kt < 2; ++kt) m = max3(max3(m, s[qt][x][kt][0], s[qt][x][kt][1]), s[qt][x][kt][2], s[qt][x][kt][3]);
        m = xmax4(m);
        const float L2E = 1.4426950408889634f;
        const float mm = m * L2E;
        f32x4 sum4 = {0, 0, 0, 0};
#pragma unroll
        for (int x = 0; x < 9; ++x)
#pragma unroll
            for (int kt = 0; kt < 2; ++kt) {
#pragma unroll
                for (int r = 0; r < 4; ++r) s[qt][x][kt][r] = __builtin_amdgcn_exp2f(__builtin_fmaf(s[qt][x][kt][r], L2E, -mm));
                sum4 += s[qt][x][kt];
            }
        float sum = xsum4((sum4[0] + sum4[1]) + (sum4[2] + sum4[3]));
        const float inv = 1.0f / sum;
#pragma unroll
        for (int x = 0; x < 9; ++x) pf[qt][x] = pack8(s[qt][x][0] * inv, s[qt][x][1] * inv);
    }
    f32x4 o[2][2];
#pragma unroll
    for (int qt = 0; qt < 2; ++qt) { o[qt][0] = (f32x4){0, 0, 0, 0}; o[qt][1] = (f32x4){0, 0, 0, 0}; }
#pragma unroll
    for (int x = 0; x < 9; ++x) {
        u32x4 v0 = *(const u32x4*)(kvb + (x * 4 + 2) * 1024);
        u32x4 v1 = *(const u32x4*)(kvb + (x * 4 + 3) * 1024);
#pragma unroll
        for (int qt = 0; qt < 2; ++qt) { o[qt][0] = mfma(v0, pf[qt][x], o[qt][0]); o[qt][1] = mfma(v1, pf[qt][x], o[qt][1]); }
    }
#pragma unroll
    for (int qt = 0; qt < 2; ++qt) {
        const unsigned row = OFF_OB + (unsigned)(32 * b0 + 16 * qt + n16) * 1152 + h * 36;
        sts4(rs, row + kq * 8, pk2(o[qt][0][0], o[qt][0][1]));
        sts4(rs, row + kq * 8 + 4, pk2(o[qt][0][2], o[qt][0][3]));
        if (kq == 0) sts4(rs, row + 32, pk2(o[qt][1][0], o[qt][1][1]));
    }
    wg_publish(p.ws, C_BA + qg * 128);
}

DI void task_bout(const Ptrs& p, rsrc_t rs, char* lds, int cg, int T) {
    const int w = __builtin_amdgcn_readfirstlane(threadIdx.x >> 6), l = tid_() & 63;
    const int n16 = l & 15, kq = l >> 4;
    u32x4 wa[18], wb[18];
    load_w2(p.bo + cg * 192, p.bo + cg * 192 + 128, 576, true, lds, w, l, wa, wb);
    cnt_wait(p.ws, C_BA + (T >> 2) * 128, 32);
    {
        u32x4 v[9];
#pragma unroll
        for (int i = 0; i < 9; ++i) { const int f = w * 9 + i, tt = f / 18, ks = f % 18; v[i] = lds16(rs, OFF_OB + (unsigned)(T * 64 + tt * 16 + n16) * 1152 + ks * 64 + kq * 16); }
#pragma unroll
        for (int i = 0; i < 9; ++i) *(u32x4*)(lds + (w * 9 + i) * 1024 + l * 16) = v[i];
    }
    bar();
    f32x4 acc[2][4];
    if (w < 4) tile_gemm<2, false>(lds + l * 16, wa, wb, acc);
    else tile_gemm<1, false>(lds + l * 16, wa, wb, acc);
#pragma unroll
    for (int i = 0; i < 2; ++i) {
        if (i == 0 || w < 4) {
            const int nt = cg * 12 + 8 * i + w;
#pragma unroll
            for (int sg = 0; sg < 2; ++sg)
                sts16(rs, OFF_BB + (unsigned)(T * 64 + (2 * sg + (kq & 1)) * 16 + n16) * 1152 + (16 * nt + 8 * (kq >> 1)) * 2, pair16(acc[i][2 * sg], acc[i][2 * sg + 1]));
        }
    }
    wg_publish(p.ws, C_BB + T * 128);
}

DI bool wave_ok(u32x4 v) {
    unsigned m = max(max(v.x, v.y), max(v.z, v.w));
    return __builtin_amdgcn_ballot_w64(m == 0xFFFFFFFFu) == 0ull;
}
DI u32x4 poll1(const char* a) {
    u32x4 v;
    for (int spins = 0;; ++spins) {
        asm volatile("global_load_dwordx4 %0, %1, off sc1\n\ts_waitcnt vmcnt(0)" : "=&v"(v) : "v"(a) : "memory");
        if (wave_ok(v) || spins > SPIN_CAP) break;
    }
    return v;
}
DI u32x4 umax4(u32x4 a, u32x4 b) { return (u32x4){max(a.x, b.x), max(a.y, b.y), max(a.z, b.z), max(a.w, b.w)}; }
template <int N>
DI void polln(const char* const (&a)[N], u32x4 (&v)[N]) {
    for (int spins = 0;; ++spins) {
        u32x4 m;
        if constexpr (N == 2) {
            asm volatile("global_load_dwordx4 %0, %2, off sc1\n\tglobal_load_dwordx4 %1, %3, off sc1\n\ts_waitcnt vmcnt(0)"
                         : "=&v"(v[0]), "=&v"(v[1]) : "v"(a[0]), "v"(a[1]) : "memory");
            m = umax4(v[0], v[1]);
        } else if constexpr (N == 3) {
            asm volatile("global_load_dwordx4 %0, %3, off sc1\n\tglobal_load_dwordx4 %1, %4, off sc1\n\tglobal_load_dwordx4 %2, %5, off sc1\n\ts_waitcnt vmcnt(0)"
                         : "=&v"(v[0]), "=&v"(v[1]), "=&v"(v[2]) : "v"(a[0]), "v"(a[1]), "v"(a[2]) : "memory");
            m = umax4(umax4(v[0], v[1]), v[2]);
        } else {
            asm volatile("global_load_dwordx4 %0, %4, off sc1\n\tglobal_load_dwordx4 %1, %5, off sc1\n\tglobal_load_dwordx4 %2, %6, off sc1\n\tglobal_load_dwordx4 %3, %7, off sc1\n\ts_waitcnt vmcnt(0)"
                         : "=&v"(v[0]), "=&v"(v[1]), "=&v"(v[2]), "=&v"(v[3]) : "v"(a[0]), "v"(a[1]), "v"(a[2]), "v"(a[3]) : "memory");
            m = umax4(umax4(v[0], v[1]), umax4(v[2], v[3]));
        }
        if (wave_ok(m) || spins > SPIN_CAP) break;
    }
}

constexpr int LA_MEM = 0;
constexpr int LA_QK = 9216;
constexpr int LA_KX = 16384;
constexpr int LA_QX = 40960;

DI void softmax12(f32x4& s0, f32x4& s1, f32x4& s2, u32x4& px, u32x2& pm) {
    const float cs = 0.08838834764831845f * 1.4426950408889634f;
    s0 *= cs; s1 *= cs; s2 *= cs;
    float m = max3(max3(max3(s0[0], s0[1], s0[2]), s0[3], s1[0]), max3(s1[1], s1[2], s1[3]), max3(max3(s2[0], s2[1], s2[2]), s2[3], s2[3]));
    m = xmax4(m);
    s0 -= m; s1 -= m; s2 -= m;
#pragma unroll
    for (int r = 0; r < 4; ++r) {
        s1[r] = __builtin_amdgcn_exp2f(s1[r]);
        s2[r] = __builtin_amdgcn_exp2f(s2[r]);
        s0[r] = __builtin_amdgcn_exp2f(s0[r]);
    }
    f32x4 t = s0 + s1 + s2;
    float sum = xsum4((t[0] + t[1]) + (t[2] + t[3]));
    const float inv = __builtin_amdgcn_rcpf(sum);
    s0 *= inv; s1 *= inv; s2 *= inv;
    px = (u32x4){nos(pk2(s1[0], s1[1])), nos(pk2(s1[2], s1[3])), nos(pk2(s2[0], s2[1])), nos(pk2(s2[2], s2[3]))};
    pm = (u32x2){nos(pk2(s0[0], s0[1])), nos(pk2(s0[2], s0[3]))};
}

DI void role_A(const Ptrs& p, rsrc_t rs, char* lds, int h) {
    const int w = __builtin_amdgcn_readfirstlane(threadIdx.x >> 6), l = tid_() & 63;
    const int n16 = l & 15, kq = l >> 4;
    const unsigned selmask = load_selmask(p.ws);
    const unsigned selM = selmask & 1u, selP0 = (selmask >> 2) & 1u, selP1 = (selmask >> 3) & 1u;
    u32x4 wq[18], wk[18];
    load_w2(p.Wq + h * 128, p.Wk + h * 128, 4096, false, lds, w, l, wq, wk);
    const int idx = (l & 8) ? 0xEEEE : 0x4444;
    const unsigned moff = amap(w * 1024 + l * 16, selM), moff8 = amap(8 * 1024 + l * 16, selM);
    const bool pfw = w >= 3 && w < 7;
    const int pq = w - 3;
    const unsigned pf_src = (pq < 2 ? OFF_KF : OFF_QF) + (unsigned)h * 8192 + (pq & 1) * 4096 + l * 16;
    char* pf_dst = lds + (pq < 2 ? LA_KX : LA_QX) + (pq & 1) * 4096 + l * 16;
    u32x4 kx[8];
    auto pf_issue = [&](int sn) {
#pragma unroll
        for (int i = 0; i < 4; ++i) kx[i] = lds16(rs, pf_src + (unsigned)sn * 32 * 8192 + i * 1024);
    };
    auto pf_write = [&](int sn) {
#pragma unroll
        for (int i = 0; i < 4; ++i) *(u32x4*)(pf_dst + (sn % 3) * 8192 + i * 1024) = kx[i];
    };
    if (pfw) { cnt_wait(p.ws, C_K, 32); pf_issue(0); pf_write(0); pf_issue(1); pf_write(1); cnt_wait(p.ws, C_K + 128, 32); }
    __builtin_amdgcn_s_waitcnt(0);
    bar();
    const float NI = -INFINITY;
    const float z0 = kq < 2 ? NI : 0.f;
    const f32x4 i0 = {z0, z0, z0, z0};
    const unsigned plx = h * 640 + (1 + kq) * 128 + (n16 & 7) * 16, plm = h * 640 + (n16 & 7) * 16;
    const unsigned px0 = amap(plx, 0), pm0 = amap(plm, 0);
    const int qkw = (16 * w + n16) * 32 + (l >> 5) * 16 + ((l >> 4) & 1) * 8;
    const int trb = LA_QK + (8 * kq + (n16 >> 2)) * 32 + (l & 3) * 8;
    u32x4 hpx = {0, 0, 0, 0}; u32x2 hpm = {0, 0};
#pragma unroll 1
    for (int s = 0; s < NSEG; ++s) {
        if (w < 3) {
#pragma unroll
            for (int i = 0; i < 8; ++i) kx[i] = *(const u32x4*)(lds + LA_KX + (s % 3) * 8192 + i * 1024 + l * 16);
        }
        const unsigned psb = p_slot(s) + (((s >> 5) ? selP1 : selP0) << 13);
        const unsigned pdx = psb + px0, pdm = psb + pm0;
        {
            const char* ms = p.ws + mem_slot(0, s);
            if (w == 7) { const char* aa[2] = {ms + moff, ms + moff8}; u32x4 v[2]; polln<2>(aa, v); *(u32x4*)(lds + LA_MEM + 7 * 1024 + l * 16) = v[0]; *(u32x4*)(lds + LA_MEM + 8 * 1024 + l * 16) = v[1]; }
            else { u32x4 a = poll1(ms + moff); *(u32x4*)(lds + LA_MEM + w * 1024 + l * 16) = a; }
        }
        bar();
        RTW(0, h, s);
        u32x4 af[9];
#pragma unroll
        for (int k2 = 0; k2 < 9; ++k2) af[k2] = *(const u32x4*)(lds + LA_MEM + k2 * 1024 + l * 16);
        SCHED();
        f32x4 aq = {0, 0, 0, 0}, ak = {0, 0, 0, 0};
#pragma unroll
        for (int k2 = 0; k2 < 9; ++k2) {
            aq = smfmac(af[k2], wq[2 * k2], wq[2 * k2 + 1], aq, idx);
            ak = smfmac(af[k2], wk[2 * k2], wk[2 * k2 + 1], ak, idx);
        }
        {
            float v[4];
#pragma unroll
            for (int r = 0; r < 4; ++r) {
                auto sw = __builtin_amdgcn_permlane32_swap(__float_as_uint(aq[r]), __float_as_uint(ak[r]), false, false);
                v[r] = __uint_as_float(sw[0]) + __uint_as_float(sw[1]);
            }
            u32x2 o = {pk2(v[0], v[1]), pk2(v[2], v[3])};
            *(u32x2*)(lds + LA_QK + qkw) = o;
        }
        bar();
        if (w == 0) {
            if (s < NSEG - 1) {
                __builtin_amdgcn_s_setprio(3);
                u32x4 F[4];
#pragma unroll
                for (int ks = 0; ks < 4; ++ks) {
                    u32x2 a = trread(lds + trb + ks * 1024);
                    u32x2 b = trread(lds + trb + ks * 1024 + 128);
                    F[ks] = (u32x4){a.x, a.y, b.x, b.y};
                }
                SCHED();
                f32x4 s0 = i0, s1 = {0, 0, 0, 0}, s2 = {0, 0, 0, 0};
#pragma unroll
                for (int ks = 0; ks < 4; ++ks) { s1 = mfma(kx[ks], F[ks], s1); s2 = mfma(kx[4 + ks], F[ks], s2); s0 = mfma(F[ks], F[ks], s0); SCHED(); }
                u32x4 px; u32x2 pm;
                softmax12(s0, s1, s2, px, pm);
                if (n16 < 8) sts16(rs, pdx, px);
                auto e0 = __builtin_amdgcn_permlane16_swap(pm.x, pm.x, false, false);
                auto e1 = __builtin_amdgcn_permlane16_swap(pm.y, pm.y, false, false);
                if (n16 < 8 && kq == 2) sts16(rs, pdm, (u32x4){e0[0], e1[0], e0[1], e1[1]});
                __builtin_amdgcn_s_setprio(0);
            }
            RT(1, h, s);
        } else if (w < 3) {
            if (s > 0) {
                if (s > 1 && l == 0) cnt_add(p.ws, C_P + ((s - 2) * 2 + (w - 1)) * 128);
                const int l2 = tid_() & 63, k2 = l2 >> 4;
                const unsigned pt = OFF_PT + (unsigned)((s - 1) * 32 + h) * 2560 + (16 * (w - 1) + (l2 & 15)) * 16;
                sts16(rs, pt + (1 + k2) * 512, hpx);
                if (k2 >= 2) sts8(rs, pt + (k2 - 2) * 8, hpm);
            }
            u32x4 F[4], Bq[4];
#pragma unroll
            for (int ks = 0; ks < 4; ++ks) {
                u32x2 a = trread(lds + trb + ks * 1024);
                u32x2 b = trread(lds + trb + ks * 1024 + 128);
                F[ks] = (u32x4){a.x, a.y, b.x, b.y};
                Bq[ks] = *(const u32x4*)(lds + LA_QX + (s % 3) * 8192 + ((w - 1) * 4 + ks) * 1024 + l * 16);
            }
            int lim = 16 * (w - 1) + n16 - 4 * kq;
            asm volatile("" : "+v"(lim));
            f32x4 s0 = i0, s1, s2;
#pragma unroll
            for (int r = 0; r < 4; ++r) { s1[r] = (r > lim) ? NI : 0.f; s2[r] = (16 + r > lim) ? NI : 0.f; }
#pragma unroll
            for (int ks = 0; ks < 4; ++ks) { s1 = mfma(kx[ks], Bq[ks], s1); s2 = mfma(kx[4 + ks], Bq[ks], s2); s0 = mfma(F[ks], Bq[ks], s0); }
            softmax12(s0, s1, s2, hpx, hpm);
        } else if (pfw) {
            if (s + 2 < NSEG) {
                u32x4 st[4];
#pragma unroll
                for (int i = 0; i < 4; ++i) st[i] = lds16(rs, pf_src + (unsigned)(s + 2) * 32 * 8192 + i * 1024);
#pragma unroll
                for (int i = 0; i < 4; ++i) *(u32x4*)(pf_dst + ((s + 2) % 3) * 8192 + i * 1024) = st[i];
                if ((s & 1) && s + 3 < NSEG) cnt_wait(p.ws, C_K + ((s + 3) >> 1) * 128, 32);
            }
        } else {
            const int l2 = tid_() & 63;
            unsigned ones = 0xFFFFFFFFu; asm volatile("" : "+v"(ones));
            if (s + PF < NSEG - 1 && l2 < 40) sts16(rs, p_slot(s + PF) + amap(h * 640 + l2 * 16, ((s + PF) >> 5) ? selP1 : selP0), (u32x4){ones, ones, ones, ones});
        }
    }
    if (w == 1 || w == 2) {
        const int l2 = tid_() & 63, k2 = l2 >> 4;
        const unsigned pt = OFF_PT + (unsigned)((NSEG - 1) * 32 + h) * 2560 + (16 * (w - 1) + (l2 & 15)) * 16;
        sts16(rs, pt + (1 + k2) * 512, hpx);
        if (k2 >= 2) sts8(rs, pt + (k2 - 2) * 8, hpm);
        wait_vm0();
        if (l == 0) { cnt_add(p.ws, C_P + ((NSEG - 2) * 2 + (w - 1)) * 128); cnt_add(p.ws, C_P + ((NSEG - 1) * 2 + (w - 1)) * 128); }
    }
}

template <int NCT, int NX>
DI void fold_unit(const Ptrs& p, rsrc_t rs, char* sm, int h, int ct0, int ks0, unsigned cnt) {
    constexpr int LD = NCT * 32 + 16, CH = NCT * 2, TOT = 128 * CH, PER = (TOT + 511) / 512;
    const int t = tid_();
    const int w = __builtin_amdgcn_readfirstlane(threadIdx.x >> 6), l = t & 63;
    const int n16 = l & 15, kq = l >> 4;
    u32x4 v[PER];
#pragma unroll
    for (int i = 0; i < PER; ++i) { int c = t + 512 * i; if (c < TOT) { int r = c / CH, cc = c % CH; v[i] = *(const u32x4*)(p.Wo + (size_t)(h * 128 + r) * 576 + ct0 * 16 + cc * 8); } }
    const int npair = w < NX ? 2 : 1;
    const u16* av = p.Wv + (size_t)(32 * (ks0 + w) + n16) * 4096 + h * 128 + 8 * kq;
    u32x4 a[2][2][4];
#pragma unroll
    for (int m = 0; m < 2; ++m)
#pragma unroll
        for (int e = 0; e < 2; ++e)
#pragma unroll
            for (int ks = 0; ks < 4; ++ks) if (m < npair) a[m][e][ks] = *(const u32x4*)(av + (size_t)(m * 256 + e * 16) * 4096 + 32 * ks);
#pragma unroll
    for (int i = 0; i < PER; ++i) { int c = t + 512 * i; if (c < TOT) { int r = c / CH, cc = c % CH; *(u32x4*)(sm + r * LD + cc * 16) = v[i]; } }
    bar();
    const int q = n16 >> 2, pp = l & 3;
    const int pn = perm16(n16), ph = pn >> 3, c = pn & 7;
    const int kqq = 2 * (kq & 1) + (kq >> 1);
    const unsigned ob = OFF_WVOF + (((unsigned)(2 * ct0 + ph) * 16 + (h >> 1)) * 18 + ks0 + w) * 1024 + ((h & 1) * 8 + c + 16 * kqq) * 16;
#pragma unroll
    for (int i = 0; i < NCT; ++i) {
        u32x4 bfr[4];
#pragma unroll
        for (int ks = 0; ks < 4; ++ks) {
            u32x2 x0 = trread(sm + (32 * ks + 8 * kq + q) * LD + i * 32 + pp * 8);
            u32x2 x1 = trread(sm + (32 * ks + 8 * kq + 4 + q) * LD + i * 32 + pp * 8);
            bfr[ks] = (u32x4){x0.x, x0.y, x1.x, x1.y};
        }
#pragma unroll
        for (int m = 0; m < 2; ++m) {
            if (m < npair) {
                f32x4 a0 = {0, 0, 0, 0}, a1 = {0, 0, 0, 0};
#pragma unroll
                for (int ks = 0; ks < 4; ++ks) { a0 = mfma(a[m][0][ks], bfr[ks], a0); a1 = mfma(a[m][1][ks], bfr[ks], a1); }
                sts16(rs, ob + ((unsigned)(2 * i) * 16 * 18 + 8 * m) * 1024, pair16(a0, a1));
            }
        }
    }
    wg_publish(p.ws, cnt);
}

constexpr int LB_MEM = 0;
constexpr int LB_T = 9216;
constexpr int LB_PART = 13312;
DI void role_B(const Ptrs& p, rsrc_t rs, char* lds, int j) {
    const int w = __builtin_amdgcn_readfirstlane(threadIdx.x >> 6), l = tid_() & 63;
    const int n16 = l & 15, kq = l >> 4;
    const unsigned selmask = load_selmask(p.ws);
    const unsigned selA = selmask & 1u, selM = (selmask >> 1) & 1u, selP0 = (selmask >> 2) & 1u, selP1 = (selmask >> 3) & 1u;
    u32x4 w0[18], w1[18];
    cnt_wait(p.ws, C_F + (j / 18) * 128, 32);
    cnt_wait(p.ws, C_F2 + (j / 24) * 128, 32);
    {
        const unsigned a = OFF_WVOF + (((unsigned)j * 16 + 2 * w) * 18) * 1024 + l * 16;
#pragma unroll
        for (int i = 0; i < 18; ++i) { w0[i] = lds16(rs, a + i * 1024); w1[i] = lds16(rs, a + (18 + i) * 1024); }
    }
    const int idx = (l & 8) ? 0xEEEE : 0x4444;
    const unsigned moff = amap(w * 1024 + l * 16, selM), moff8 = amap(8 * 1024 + l * 16, selM);
    const int row = l & 7, bb = (l >> 3) & 1;
    const int G = j >> 1, ph = j & 1;
    const int up0 = w < 4 ? w + 4 : w - 4, up1 = w < 4 ? w + 12 : w + 4;
    const unsigned lo_m = (8 * (w & 3) + 4 * bb + kq) * 640 + row * 16;
    const unsigned lo_0 = (2 * up0 + bb) * 640 + (1 + kq) * 128 + row * 16;
    const unsigned lo_1 = (2 * up1 + bb) * 640 + (1 + kq) * 128 + row * 16;
    const unsigned soff = amap(j * 128 + row * 16, kq ? selM : selA) + (kq ? MB2 : 0u);
    const int thead = 4 * w + 2 * (l >> 5) + (n16 >> 3);
    const int toff = (thead * 8 + (l & 7)) * 16 + ((l >> 4) & 1) * 8;
    const unsigned tgo = OFF_TF + ((unsigned)G * 32 + thead) * 256 + (ph * 8 + (l & 7)) * 16 + ((l >> 4) & 1) * 8;
    const unsigned ubase = OFF_UF + ((unsigned)bb * 36 + G) * 1024 + (ph * 8 + row + 16 * kq) * 16;
    cnt_wait(p.ws, C_U, 32);
    __builtin_amdgcn_s_waitcnt(0);
#pragma unroll 1
    for (int s = 0; s < NSEG; ++s) {
        {
            const char* ms = p.ws + mem_slot(1, s);
            if (w == 7) { const char* aa[2] = {ms + moff, ms + moff8}; u32x4 v[2]; polln<2>(aa, v); *(u32x4*)(lds + LB_MEM + 7 * 1024 + l * 16) = v[0]; *(u32x4*)(lds + LB_MEM + 8 * 1024 + l * 16) = v[1]; }
            else { u32x4 a = poll1(ms + moff); *(u32x4*)(lds + LB_MEM + w * 1024 + l * 16) = a; }
        }
        bar();
        RTW(4, j, s);
        u32x4 uf0 = lds16(rs, ubase + ((unsigned)s * 32 * 36 + up0 * 72) * 1024);
        u32x4 uf1 = lds16(rs, ubase + ((unsigned)s * 32 * 36 + up1 * 72) * 1024);
        if (w == 2 && s + 1 + PF < NSEG && bb == 0 && kq < 2) sts16(rs, mem_slot(0, s + 1 + PF) + soff, SENT);
        u32x4 af[9];
#pragma unroll
        for (int k2 = 0; k2 < 9; ++k2) af[k2] = *(const u32x4*)(lds + LB_MEM + k2 * 1024 + l * 16);
        SCHED();
        f32x4 a0 = {0, 0, 0, 0}, a1 = {0, 0, 0, 0};
#pragma unroll
        for (int k2 = 0; k2 < 9; ++k2) {
            a0 = smfmac(af[k2], w0[2 * k2], w0[2 * k2 + 1], a0, idx);
            a1 = smfmac(af[k2], w1[2 * k2], w1[2 * k2 + 1], a1, idx);
        }
        {
            float v[4];
#pragma unroll
            for (int r = 0; r < 4; ++r) {
                auto sw = __builtin_amdgcn_permlane32_swap(__float_as_uint(a0[r]), __float_as_uint(a1[r]), false, false);
                v[r] = __uint_as_float(sw[0]) + __uint_as_float(sw[1]);
            }
            u32x2 o = {pk2(v[0], v[1]), pk2(v[2], v[3])};
            *(u32x2*)(lds + LB_T + toff) = o;
            sts8(rs, tgo + (unsigned)s * T_SLOT, o);
        }
        if (s == NSEG - 1) break;
        if (s & 1) cnt_wait(p.ws, C_U + ((s + 1) >> 1) * 128, 32);
        __builtin_amdgcn_s_waitcnt(0);
        asm volatile("" : "+v"(uf0), "+v"(uf1) :: "memory");
        bar();
        const char* ps = p.ws + p_slot(s);
        const unsigned sp = (s >> 5) ? selP1 : selP0;
        const unsigned mdst = mem_slot(0, s + 1) + soff;
        f32x4 acc = {0, 0, 0, 0};
        RTW(6, j, s);
        if (w < 4) {
            u32x4 tf = *(const u32x4*)(lds + LB_T + ((8 * w + 4 * bb + kq) * 8 + row) * 16);
            const char* aa[3] = {ps + amap(lo_0, sp), ps + amap(lo_1, sp), ps + amap(lo_m, sp)};
            u32x4 v[3];
            polln<3>(aa, v);
            acc = mfma(uf0, v[0], acc);
            acc = mfma(uf1, v[1], acc);
            acc = mfma(tf, v[2], acc);
        } else {
            const char* aa[2] = {ps + amap(lo_0, sp), ps + amap(lo_1, sp)};
            u32x4 v[2];
            polln<2>(aa, v);
            acc = mfma(uf0, v[0], acc);
            acc = mfma(uf1, v[1], acc);
        }
        if (bb == (l >> 5)) *(f32x4*)(lds + LB_PART + (((w * 2 + bb) * 8 + row) * 2 + (kq & 1)) * 16) = acc;
        bar();
        RTW(2, j, s);
        if (w == 0) {
            f32x4 t0 = *(const f32x4*)(lds + LB_PART + (((4 * kq + 0) * 8 + row) * 2 + bb) * 16);
            f32x4 t1 = *(const f32x4*)(lds + LB_PART + (((4 * kq + 1) * 8 + row) * 2 + bb) * 16);
            f32x4 t2 = *(const f32x4*)(lds + LB_PART + (((4 * kq + 2) * 8 + row) * 2 + bb) * 16);
            f32x4 t3 = *(const f32x4*)(lds + LB_PART + (((4 * kq + 3) * 8 + row) * 2 + bb) * 16);
            f32x4 t = (t0 + t1) + (t2 + t3);
#pragma unroll
            for (int r = 0; r < 4; ++r) t[r] = xsum4(t[r]);
            unsigned o0 = nos(pk2(t[0], t[1])), o1 = nos(pk2(t[2], t[3]));
            unsigned h0 = __builtin_amdgcn_update_dpp(0u, o0, 0x108  , 0xf, 0xf, false);
            unsigned h1 = __builtin_amdgcn_update_dpp(0u, o1, 0x108, 0xf, 0xf, false);
            u32x4 o = {o0, o1, h0, h1};
            if (bb == 0 && kq < 2) sts16(rs, mdst, o);
        }
        RT(3, j, s);
    }
    wg_publish(p.ws, C_TL);
}

template <int NC>
DI void task_post(const Ptrs& p, rsrc_t rs, char* lds, int s, int ct0) {
    const int w = __builtin_amdgcn_readfirstlane(threadIdx.x >> 6), l = tid_() & 63;
    const int n16 = l & 15, kq = l >> 4;
    f32x4 aa[NC][2];
#pragma unroll
    for (int i = 0; i < NC; ++i) { aa[i][0] = aa[i][1] = (f32x4){0, 0, 0, 0}; }
    const unsigned pt = OFF_PT + (unsigned)s * 32 * 2560;
    const unsigned uf = OFF_UF + (unsigned)s * 32 * 36 * 1024;
    cnt_wait(p.ws, C_U + (s >> 1) * 128, 32);
    u32x4 yu[4][NC];
#pragma unroll
    for (int hh = 0; hh < 4; ++hh)
#pragma unroll
        for (int i = 0; i < NC; ++i) { const int h = 4 * w + hh; yu[hh][i] = lds16(rs, uf + ((unsigned)h * 36 + ct0 + i) * 1024 + l * 16); }
    const int t = tid_(), tok = t & 31, q = t >> 5, ct = q >> 2, m = q & 3;
    cnt_wait(p.ws, C_BB + (s >> 1) * 128, 3);
    u32x2 bv = {0, 0};
    if (ct < NC) bv = __builtin_amdgcn_raw_buffer_load_b64(rs, OFF_BB + (unsigned)(32 * s + tok) * 1152 + (16 * (ct0 + ct) + 4 * m) * 2, 0, 16);
    const float g = ((const float*)(p.ws + OFF_G))[32 * s + tok];
    cnt_wait(p.ws, C_P + (s * 2) * 128, 32);
    cnt_wait(p.ws, C_P + (s * 2 + 1) * 128, 32);
    if (s == NSEG - 1) cnt_wait(p.ws, C_TL, 72);
    {
        u32x4 pb[5][2], y[NC];
#pragma unroll
        for (int i = 0; i < NC; ++i) y[i] = lds16(rs, OFF_TF + (unsigned)s * T_SLOT + ((unsigned)(ct0 + i) * 32 + 4 * w + kq) * 256 + n16 * 16);
#pragma unroll
        for (int tt = 0; tt < 2; ++tt) pb[4][tt] = lds16(rs, pt + (4 * w + kq) * 2560 + (16 * tt + n16) * 16);
#pragma unroll
        for (int hh = 0; hh < 4; ++hh)
#pragma unroll
            for (int tt = 0; tt < 2; ++tt) pb[hh][tt] = lds16(rs, pt + (4 * w + hh) * 2560 + (1 + kq) * 512 + (16 * tt + n16) * 16);
#pragma unroll
        for (int hh = 0; hh < 4; ++hh)
#pragma unroll
            for (int i = 0; i < NC; ++i)
#pragma unroll
                for (int tt = 0; tt < 2; ++tt) aa[i][tt] = mfma(yu[hh][i], pb[hh][tt], aa[i][tt]);
#pragma unroll
        for (int i = 0; i < NC; ++i) {
#pragma unroll
            for (int tt = 0; tt < 2; ++tt) aa[i][tt] = mfma(y[i], pb[4][tt], aa[i][tt]);
        }
    }
#pragma unroll
    for (int i = 0; i < NC; ++i)
#pragma unroll
        for (int tt = 0; tt < 2; ++tt) *(f32x4*)(lds + ((w * NC + i) * 2 + tt) * 1024 + l * 16) = aa[i][tt];
    bar();
    if (ct < NC) {
        const int tt = tok >> 4, t16 = tok & 15;
        const int k0 = m >> 1, r0 = 2 * (m & 1);
        f32x2 a01 = {0, 0}, a23 = {0, 0};
#pragma unroll
        for (int ww = 0; ww < 8; ++ww) {
            const char* ba = lds + ((ww * NC + ct) * 2 + tt) * 1024;
            a01 += *(const f32x2*)(ba + (t16 + 16 * k0) * 16 + r0 * 4);
            a23 += *(const f32x2*)(ba + (t16 + 16 * (k0 + 2)) * 16 + r0 * 4);
        }
        const float og = rbf(1.f - g);
        float o0 = rbf(g * rbf(a01[0])) + rbf(og * __uint_as_float(bv.x << 16));
        float o1 = rbf(g * rbf(a01[1])) + rbf(og * __uint_as_float(bv.x & 0xffff0000u));
        float o2 = rbf(g * rbf(a23[0])) + rbf(og * __uint_as_float(bv.y << 16));
        float o3 = rbf(g * rbf(a23[1])) + rbf(og * __uint_as_float(bv.y & 0xffff0000u));
        *(u32x2*)((char*)p.out + (size_t)(32 * s + tok) * 1152 + (16 * (ct0 + ct) + 4 * m) * 2) = (u32x2){pk2(o0, o1), pk2(o2, o3)};
    }
    bar();
}

__global__ void __launch_bounds__(512, 2) k_mega(Ptrs pin) {
    extern __shared__ __attribute__((aligned(16))) char lds[];
    Ptrs p;
    p.x = uni(pin.x); p.mem0 = uni(pin.mem0); p.Wq = uni(pin.Wq); p.Wk = uni(pin.Wk); p.Wv = uni(pin.Wv); p.Wo = uni(pin.Wo);
    p.bw = uni(pin.bw); p.bo = uni(pin.bo); p.gw = uni(pin.gw); p.out = uni(pin.out); p.ws = uni(pin.ws);
    unsigned* coff = (unsigned*)(p.ws + C_OFF);
    if (blockIdx.x == 0 && threadIdx.x == 0) __hip_atomic_store(coff, xcc_id() + 1u, __ATOMIC_RELAXED, __HIP_MEMORY_SCOPE_AGENT);
    const rsrc_t rs = mk_rsrc(p.ws);
    RT(5, blockIdx.x, 0);
    unsigned off = 0;
    for (int spins = 0; spins < (1 << 20); ++spins) { off = __hip_atomic_load(coff, __ATOMIC_RELAXED, __HIP_MEMORY_SCOPE_AGENT); if (off) break; }
    off = __builtin_amdgcn_readfirstlane(off) - 1u;
    const int g = (blockIdx.x + off) & 7, i = blockIdx.x >> 3;
    RT(5, blockIdx.x, 1);
    if (g == 0) role_A(p, rs, lds, i);
    else if (g < 4) {
        const int k = (g - 1) * 32 + i;
        fold_unit<12, 0>(p, rs, lds, k / 3, (k % 3) * 12, 10, C_F2 + (k % 3) * 128);
        RT(5, blockIdx.x, 2);
        if (i < 24) role_B(p, rs, lds, (g - 1) * 24 + i);
    }
    else {
        const int h = 8 * (g - 4) + (i >> 2), sub = i & 3;
        const int widx = i * 4 + (g - 4);
        fold_unit<9, 2>(p, rs, lds, h, sub * 9, 0, C_F + sub * 128);
        if (sub < 2) worker_qk(p, rs, lds, h, sub); else worker_vu(p, rs, lds, h, sub & 1);
        RT(5, blockIdx.x, 2);
        if (widx < 72) { const int cg = widx % 9, tg = widx / 9; if (cg < 6) bgemm_body<false>(p, rs, lds, cg, tg); else bgemm_body<true>(p, rs, lds, cg, tg); }
        RT(5, blockIdx.x, 3);
        for (int t = (widx + 56) & 127; t < 256; t += 128) task_battn(p, rs, lds, t >> 5, t & 31);
        RT(5, blockIdx.x, 4);
        if (widx < 96) task_bout(p, rs, lds, widx % 3, widx / 3);
        RT(5, blockIdx.x, 5);
        for (int t = widx; t < 576; t += 128) task_post<4>(p, rs, lds, t / 9, (t % 9) * 4);
    }
    RT(5, blockIdx.x, 63);
}

extern "C" void kernel_launch(void* const* d_in, const int* in_sizes, int n_in,
                              void* d_out, int out_size, void* d_ws, size_t ws_size,
                              hipStream_t stream) {
    Ptrs p;
    p.x = (const u16*)d_in[0]; p.mem0 = (const u16*)d_in[1];
    p.Wq = (const u16*)d_in[2]; p.Wk = (const u16*)d_in[3]; p.Wv = (const u16*)d_in[4]; p.Wo = (const u16*)d_in[5];
    p.bw = (const u16*)d_in[8]; p.bo = (const u16*)d_in[9]; p.gw = (const u16*)d_in[10];
    p.out = (u16*)d_out;
    p.ws = (char*)(((uintptr_t)d_ws + MB2 - 1) & ~(uintptr_t)(MB2 - 1));
    (void)hipFuncSetAttribute((const void*)k_mega, hipFuncAttributeMaxDynamicSharedMemorySize, XL_BYTES);
    k_init<<<NB_INIT + 1, 256, 0, stream>>>(p);
    k_mega<<<256, 512, XL_BYTES, stream>>>(p);
}
```

```cpp
#include <hip/hip_runtime.h>
#include <stdint.h>

typedef unsigned short u16;
typedef __bf16 bf16x8 __attribute__((ext_vector_type(8)));
typedef __bf16 bf16x16 __attribute__((ext_vector_type(16)));
typedef __bf16 bf16x2 __attribute__((ext_vector_type(2)));
typedef float f32x4 __attribute__((ext_vector_type(4)));
typedef float f32x2 __attribute__((ext_vector_type(2)));
typedef unsigned u32x4 __attribute__((ext_vector_type(4)));
typedef unsigned u32x2 __attribute__((ext_vector_type(2)));
typedef unsigned u32x8 __attribute__((ext_vector_type(8)));
typedef short s16x4 __attribute__((ext_vector_type(4)));
typedef __amdgpu_buffer_rsrc_t rsrc_t;
#define LAS __attribute__((address_space(3)))
#define DI __device__ __forceinline__
#define SCHED() __builtin_amdgcn_sched_barrier(0)

DI float bf2f(u16 v) { return __uint_as_float(((unsigned)v) << 16); }
DI unsigned pk2(float a, float b) { f32x2 f = {a, b}; bf16x2 h = __builtin_convertvector(f, bf16x2); return __builtin_bit_cast(unsigned, h); }
DI float rbf(float f) { return __uint_as_float(pk2(f, 0.f) << 16); }
DI f32x4 mfma(u32x4 a, u32x4 b, f32x4 c) { return __builtin_amdgcn_mfma_f32_16x16x32_bf16(__builtin_bit_cast(bf16x8, a), __builtin_bit_cast(bf16x8, b), c, 0, 0, 0); }
DI f32x4 smfmac(u32x4 a, u32x4 b0, u32x4 b1, f32x4 c, int idx) {
    u32x8 b = __builtin_shufflevector(b0, b1, 0, 1, 2, 3, 4, 5, 6, 7);
    return __builtin_amdgcn_smfmac_f32_16x16x64_bf16(__builtin_bit_cast(bf16x8, a), __builtin_bit_cast(bf16x16, b), c, idx, 0, 0);
}
DI u32x2 trread(const void* lds_ptr) {
    s16x4 v = __builtin_amdgcn_ds_read_tr16_b64_v4i16((LAS s16x4*)(lds_ptr));
    return __builtin_bit_cast(u32x2, v);
}
DI int tid_() { int t = threadIdx.x; asm volatile("" : "+v"(t)); return t; }
DI float max2(float a, float b) { float r; asm("v_max_f32 %0, %1, %2" : "=v"(r) : "v"(a), "v"(b)); return r; }
DI float max3(float a, float b, float c) { float r; asm("v_max3_f32 %0, %1, %2, %3" : "=v"(r) : "v"(a), "v"(b), "v"(c)); return r; }

#define CSH 0
#define PGRP 4
#define MBSIDE 0u
#define TSIDE 1u
#define PSIDE 1u
#define CSIDE 1u
#define WFK 10
#define RING 8
#define CALIT 3
#define ADN 4
#define AUP 32
#define AUP2 64
#define M0 800
#define MHI 1600
#define GUARD 2.0f
#define A0 3000
#define AHI 3800
#define B0 2000
#define BHI 2400
constexpr int NSEG = 64;
constexpr unsigned MB2 = 2u << 20;
constexpr unsigned OFF_HAND = 0;
constexpr int NPAGE = 4;
constexpr unsigned OFF_R = NPAGE * MB2;
constexpr unsigned SZ_F = 96u << 20;
constexpr unsigned SZ_UF = 64u * 72 * 16384;
constexpr unsigned SZ_WVO = 72u * 16 * 18 * 1024;
constexpr unsigned T_SLOT = 36u * 32 * 256;
constexpr unsigned SZ_QF = 64u * 32 * 8192;
constexpr unsigned SZ_PT = 64u * 32 * 2560;
constexpr unsigned OFF_TF = SZ_UF;
constexpr unsigned OFF_PT = OFF_TF + 64u * T_SLOT;
constexpr unsigned OFF_QF = 0;
constexpr unsigned OFF_KF = OFF_QF + SZ_QF;
static_assert(OFF_PT + SZ_PT <= SZ_F && OFF_KF + SZ_QF <= SZ_UF, "spaces");
constexpr unsigned OFF_CTL = OFF_R + 2 * SZ_F;
constexpr unsigned OFF_G = OFF_CTL + 196608;
constexpr unsigned OFF_XF = OFF_CTL + 262144;
constexpr unsigned SZ_XF = 2048u * 576 * 2;
constexpr unsigned OFF_WVOF = OFF_XF + SZ_XF;
constexpr unsigned OFF_QB = OFF_WVOF + SZ_WVO;
constexpr unsigned SZ_QB = 2048u * 1152 + 4096;
constexpr unsigned OFF_KB = OFF_QB + SZ_QB;
constexpr unsigned OFF_VB = OFF_KB + SZ_QB;
constexpr unsigned SZ_VB = 32u * 64 * 2048;
constexpr unsigned OFF_OB = OFF_VB + SZ_VB;
constexpr unsigned SZ_OB = 2048u * 576 * 2;
constexpr unsigned OFF_BB = OFF_OB + SZ_OB;
constexpr unsigned WS_END = OFF_BB + SZ_OB;
static_assert((size_t)WS_END + 2 * MB2 <= (256u << 20), "ws too big");
constexpr unsigned C_SEL = OFF_CTL;
constexpr unsigned C_OFF = OFF_CTL + 256 + CSH;
constexpr unsigned C_BASE = OFF_CTL + 16384;
constexpr unsigned C_K = 128;
constexpr unsigned C_U = C_K + 32 * 128;
constexpr unsigned C_BG = C_U + 32 * 128;
constexpr unsigned C_BA = C_BG + 8 * 128;
constexpr unsigned C_BB = C_BA + 8 * 128;
constexpr unsigned C_P = C_BB + 32 * 128;
constexpr unsigned C_F = C_P + 128 * 128;
constexpr unsigned C_F2 = C_F + 4 * 128;
constexpr unsigned C_END = C_F2 + 4 * 128;
static_assert(C_BASE + 2 * (C_END + 8192) <= OFF_G && (OFF_CTL / MB2) % 2 == 0 && OFF_CTL % MB2 == 0, "ctl");
constexpr unsigned FARU = 1u;
constexpr int NTAIL = 3;
constexpr int PF = 3;

DI int perm16(int d16) { return ((d16 >> 1) & 1) * 8 + (d16 >> 2) * 2 + (d16 & 1); }

struct Ptrs {
    const u16* x; const u16* mem0; const u16* Wq; const u16* Wk; const u16* Wv; const u16* Wo;
    const u16* bw; const u16* bo; const u16* gw; u16* out; char* ws;
};
struct Ctx : Ptrs { unsigned sel; };

template <typename T> DI T* uni(T* q) {
    unsigned long long v = (unsigned long long)q;
    unsigned lo = __builtin_amdgcn_readfirstlane((unsigned)v), hi = __builtin_amdgcn_readfirstlane((unsigned)(v >> 32));
    return (T*)(__attribute__((address_space(1))) T*)(((unsigned long long)hi << 32) | lo);
}
DI rsrc_t mk_rsrc(char* ws) { return __builtin_amdgcn_make_buffer_rsrc((void*)ws, 0, 0x7ffffff0, 0x00020000); }
DI u32x4 lds16(rsrc_t r, unsigned off) { return __builtin_amdgcn_raw_buffer_load_b128(r, off, 0, 16); }
DI void sts16(rsrc_t r, unsigned off, u32x4 v) { __builtin_amdgcn_raw_buffer_store_b128(v, r, off, 0, 16); }
DI void sts8(rsrc_t r, unsigned off, u32x2 v) { __builtin_amdgcn_raw_buffer_store_b64(v, r, off, 0, 16); }
DI void sts4(rsrc_t r, unsigned off, unsigned v) { __builtin_amdgcn_raw_buffer_store_b32(v, r, off, 0, 16); }
DI void wait_vm0() { asm volatile("s_waitcnt vmcnt(0)" ::: "memory"); }
DI void bar() { asm volatile("s_waitcnt lgkmcnt(0)" ::: "memory"); __builtin_amdgcn_s_barrier(); asm volatile("" ::: "memory"); }
DI unsigned cmap(const Ctx& p, unsigned o) { return C_BASE + ((o >> 13) << 14) + (((p.sel ^ CSIDE) & 1u) << 13) + (o & 8191u); }
DI void cnt_add(const Ctx& p, unsigned off) { __hip_atomic_fetch_add((unsigned*)(p.ws + cmap(p, off)), 1u, __ATOMIC_RELAXED, __HIP_MEMORY_SCOPE_AGENT); }
constexpr int SPIN_CAP = 1 << 17;
DI void cnt_wait(const Ctx& p, unsigned off, unsigned target) {
    char* ws = p.ws; off = cmap(p, off);
    for (int spins = 0; spins < SPIN_CAP; ++spins) {
        unsigned v = __hip_atomic_load((unsigned*)(ws + off), __ATOMIC_RELAXED, __HIP_MEMORY_SCOPE_AGENT);
        if ((unsigned)__builtin_amdgcn_readfirstlane(v) >= target) break;
        __builtin_amdgcn_s_sleep(4);
    }
    asm volatile("" ::: "memory");
}
DI void wg_publish(const Ctx& p, unsigned off) {
    wait_vm0();
    bar();
    if (threadIdx.x == 0) cnt_add(p, off);
}

DI float xmax4(float x) {
    auto a = __builtin_amdgcn_permlane16_swap(__float_as_uint(x), __float_as_uint(x), false, false);
    x = max2(__uint_as_float(a[0]), __uint_as_float(a[1]));
    auto b = __builtin_amdgcn_permlane32_swap(__float_as_uint(x), __float_as_uint(x), false, false);
    return max2(__uint_as_float(b[0]), __uint_as_float(b[1]));
}
DI float xsum4(float x) {
    auto a = __builtin_amdgcn_permlane16_swap(__float_as_uint(x), __float_as_uint(x), false, false);
    x = __uint_as_float(a[0]) + __uint_as_float(a[1]);
    auto b = __builtin_amdgcn_permlane32_swap(__float_as_uint(x), __float_as_uint(x), false, false);
    return __uint_as_float(b[0]) + __uint_as_float(b[1]);
}

constexpr unsigned MEM_SLOT = 32768;
constexpr unsigned P_SLOT = 49152;
DI unsigned amap(unsigned o, unsigned sel) { return ((o >> 13) << 14) + (sel << 13) + (o & 8191u); }
DI unsigned mem_slot(int copy, int s) { return OFF_HAND + (unsigned)copy * MB2 + (unsigned)(s & (RING - 1)) * MEM_SLOT; }
DI unsigned p_slot(int s) { return OFF_HAND + 2u * MB2 + (unsigned)(s & (RING - 1)) * P_SLOT; }
DI unsigned rmap(unsigned o, unsigned b) { return OFF_R + ((o >> 13) << 14) + (((b ^ (o >> 20)) & 1u) << 13) + (o & 8191u); }
DI unsigned load_selmask(const char* ws) { const int l = threadIdx.x & 63; unsigned v = l < NPAGE ? ((const unsigned*)(ws + C_SEL))[l] : 0u; return (unsigned)__builtin_amdgcn_ballot_w64(v != 0u); }
DI unsigned nos(unsigned v) { return min(v, 0xFFFFFFFEu); }
DI unsigned long long clk() { unsigned long long t; asm volatile("s_memtime %0\n\ts_waitcnt lgkmcnt(0)" : "=s"(t) :: "memory"); return t; }
DI unsigned xcc_id() { return (unsigned)__builtin_amdgcn_s_getreg(20 | (3 << 11)) & 0xfu; }

constexpr int NB_INIT = 288;
#define SENT ((u32x4){0xFFFFFFFFu, 0xFFFFFFFFu, 0xFFFFFFFFu, 0xFFFFFFFFu})
DI void init_block(const Ptrs& p, int b) {
    const int tid = b * 256 + threadIdx.x, nth = NB_INIT * 256;
    {
        u32x4 v[2];
#pragma unroll
        for (int k = 0; k < 2; ++k) {
            const int c = tid + k * nth, l = c & 63, f = c >> 6, T = f / 72, r = f % 72, tt = r / 18, ks = r % 18;
            v[k] = *(const u32x4*)(p.x + (size_t)(T * 64 + tt * 16 + (l & 15)) * 576 + 32 * ks + 8 * (l >> 4));
        }
#pragma unroll
        for (int k = 0; k < 2; ++k) *(u32x4*)(p.ws + OFF_XF + (unsigned)(tid + k * nth) * 16) = v[k];
    }
    for (int i = tid; i < 2 * 2 * (PF + 1) * 576; i += nth) {
        const int c = i % 576, r = i / 576, s = r % (PF + 1), copy = (r / (PF + 1)) & 1, sel = r / (2 * (PF + 1));
        u32x4 v = SENT;
        if (s == 0) {
            const int j = c >> 3, row = c & 7, G = j >> 1, ph = j & 1;
            const unsigned* m = (const unsigned*)(p.mem0 + row * 576 + 16 * G);
            v = (u32x4){nos(m[ph]), nos(m[2 + ph]), nos(m[4 + ph]), nos(m[6 + ph])};
        }
        *(u32x4*)(p.ws + mem_slot(copy, s) + amap(c * 16, sel)) = v;
    }
    for (int i = tid; i < 2 * PF * 1280; i += nth) {
        const int c = i % 1280, r = i / 1280, s = r % PF, sel = r / PF;
        *(u32x4*)(p.ws + p_slot(s) + amap(c * 16, sel)) = SENT;
    }
    for (int i = tid; i < 2 * NTAIL * 32 * 2560 / 16; i += nth) *(u32x4*)(p.ws + rmap(OFF_PT + (unsigned)(NSEG - NTAIL) * 32 * 2560 + (unsigned)(i >> 1) * 16, i & 1)) = SENT;
    for (int i = tid; i < (int)(2 * NTAIL * T_SLOT / 16); i += nth) *(u32x4*)(p.ws + rmap(OFF_TF + (unsigned)(NSEG - NTAIL) * T_SLOT + (unsigned)(i >> 1) * 16, i & 1)) = SENT;
    for (int i = tid; i < (int)(2 * (C_END + 8192) / 16); i += nth) *(u32x4*)(p.ws + C_BASE + i * 16) = (u32x4){0, 0, 0, 0};
    if (tid == 0) *(u32x4*)(p.ws + C_OFF) = (u32x4){0, 0, 0, 0};
    const int wv = tid >> 6, l = threadIdx.x & 63, nw = nth >> 6;
    for (int t = wv; t < 2048; t += nw) {
        float acc = 0.f;
#pragma unroll
        for (int i = 0; i < 9; ++i) acc += rbf(bf2f(p.x[(size_t)t * 576 + l + 64 * i]) * bf2f(p.gw[l + 64 * i]));
#pragma unroll
        for (int o = 32; o > 0; o >>= 1) acc += __shfl_xor(acc, o);
        const float sg = rbf(acc);
        const float g = rbf(1.f / (1.f + __expf(-sg)));
        if (l == 0) ((float*)(p.ws + OFF_G))[t] = g;
    }
}
DI void calib_block(const Ptrs& p) {
    const int pg = blockIdx.x * 4 + (threadIdx.x >> 6), l = threadIdx.x & 63;
    const unsigned iod = xcc_id() >> 2;
    unsigned best[2] = {1u << 30, 1u << 30};
    const unsigned base = OFF_HAND + pg * MB2 + (pg < 2 ? 63u * MEM_SLOT + 16384u + 4096u : 32u * P_SLOT + 4096u);
    for (int it = 0; it < CALIT; ++it) {
#pragma unroll
        for (int c = 0; c < 2; ++c) {
            char* a = p.ws + base + c * 8192 + (l & 7) * 16;
            u32x4 v = {1, 2, 3, 4}, r;
            unsigned long long t0 = clk();
            asm volatile("global_store_dwordx4 %0, %1, off sc1\n\ts_waitcnt vmcnt(0)" :: "v"(a), "v"(v) : "memory");
            unsigned long long t1 = clk();
            asm volatile("global_load_dwordx4 %0, %1, off sc1\n\ts_waitcnt vmcnt(0)" : "=&v"(r) : "v"(a) : "memory");
            unsigned long long t2 = clk();
            best[c] = min(best[c], min((unsigned)(t2 - t1), (unsigned)(t1 - t0)));
        }
    }
    __shared__ int dd[4];
    if (l == 0) dd[pg] = (int)best[0] - (int)best[1];
    __syncthreads();
    int vote = 0;
#pragma unroll
    for (int i = 0; i < 4; ++i) { const int d = min(max(dd[i], -250), 250); vote += (i & 1) ? -d : d; }
    unsigned near = ((vote > 0) ? 1u : 0u) ^ (unsigned)(pg & 1);
    if (vote < 300 && vote > -300) near = dd[pg] > 0 ? 1u : 0u;
    if (l == 0) ((unsigned*)(p.ws + C_SEL))[pg] = near ^ iod;
}
__global__ void __launch_bounds__(256) k_init(Ptrs p) {
    if (blockIdx.x == 0) calib_block(p); else init_block(p, blockIdx.x - 1);
}


constexpr int XL_BYTES = 8 * 18 * 1024;
constexpr int XBUF = 4 * 18 * 1024;

DI void dma16(const void* sbase, unsigned voff, unsigned ldsa) {
    unsigned keep;
    asm volatile("s_mov_b32 %0, m0\n\ts_mov_b32 m0, %3\n\ts_nop 0\n\tglobal_load_lds_dwordx4 %1, %2\n\ts_mov_b32 m0, %0" : "=&s"(keep) : "v"(voff), "s"(sbase), "s"(ldsa) : "memory");
}
DI unsigned lds_addr(const char* lds) { return __builtin_amdgcn_readfirstlane((unsigned)(size_t)(LAS const char*)lds); }
#define WAITV(n) asm volatile("s_waitcnt vmcnt(" #n ")" ::: "memory")

DI unsigned wblk_lane_off(int N, int w, int l, bool half) {
    const int r = l >> 4, ch0 = (l & 15) ^ ((r << 2) | (w & 3));
    const int ch = half ? (ch0 & 7) : ch0;
    return (unsigned)((4 * w + r) * N + ch * 8) * 2u;
}
DI u32x4 wblk_frag(const char* blk, int c, int l) {
    const int g = l >> 4, q = (l & 15) >> 2, pp = l & 3;
    u32x2 v[2];
#pragma unroll
    for (int t = 0; t < 2; ++t) {
        const int row = 8 * g + 4 * t + q, ch = 2 * c + (pp >> 1);
        v[t] = trread(blk + 256 * row + 16 * (ch ^ (((row & 3) << 2) | ((row >> 2) & 3))) + 8 * (pp & 1));
    }
    return (u32x4){v[0].x, v[0].y, v[1].x, v[1].y};
}
DI void load_w2(const u16* src0, const u16* src1, int N, bool half1, char* lds, int w, int l, u32x4 (&wa)[18], u32x4 (&wb)[18]) {
    const unsigned la = lds_addr(lds);
    const unsigned o0 = wblk_lane_off(N, w, l, false), o1 = wblk_lane_off(N, w, l, half1);
#pragma unroll
    for (int c = 0; c < 2; ++c) {
#pragma unroll
        for (int k = 0; k < 9; ++k) {
            dma16(src0 + (size_t)(32 * (9 * c + k)) * N, o0, la + (2 * k) * 8192 + w * 1024);
            dma16(src1 + (size_t)(32 * (9 * c + k)) * N, o1, la + (2 * k + 1) * 8192 + w * 1024);
        }
        WAITV(0);
        bar();
#pragma unroll
        for (int k = 0; k < 9; ++k) {
            wa[9 * c + k] = wblk_frag(lds + (2 * k) * 8192, w, l);
            wb[9 * c + k] = wblk_frag(lds + (2 * k + 1) * 8192, half1 ? (w & 3) : w, l);
        }
        bar();
    }
}
DI void dma_x(const char* ws, int T, unsigned la, int w, int l) {
#pragma unroll
    for (int i = 0; i < 9; ++i) dma16(ws + OFF_XF + (size_t)(T * 72 + w * 9 + i) * 1024, (unsigned)l * 16u, la + (w * 9 + i) * 1024);
}
template <int NW, bool VPART, int NT = 4>
DI void tile_gemm(const char* xb, const u32x4 (&wa)[18], const u32x4 (&wb)[18], f32x4 (&acc)[2][4], const char* nsrc = nullptr, unsigned nlds = 0, unsigned voff = 0) {
    u32x4 xf[2][4];
#pragma unroll
    for (int t = 0; t < NT; ++t) { xf[0][t] = *(const u32x4*)(xb + (t * 18) * 1024); acc[0][t] = (f32x4){0, 0, 0, 0}; acc[1][t] = (f32x4){0, 0, 0, 0}; }
#pragma unroll
    for (int ks = 0; ks < 18; ++ks) {
        if (ks + 1 < 18) {
#pragma unroll
            for (int t = 0; t < NT; ++t) xf[(ks + 1) & 1][t] = *(const u32x4*)(xb + (t * 18 + ks + 1) * 1024);
        }
        SCHED();
#pragma unroll
        for (int t = 0; t < NT; ++t) {
            acc[0][t] = VPART ? mfma(xf[ks & 1][t], wa[ks], acc[0][t]) : mfma(wa[ks], xf[ks & 1][t], acc[0][t]);
            if (NW == 2) acc[1][t] = VPART ? mfma(xf[ks & 1][t], wb[ks], acc[1][t]) : mfma(wb[ks], xf[ks & 1][t], acc[1][t]);
        }
        SCHED();
        if (ks < 9 && nsrc) dma16(nsrc + ks * 1024, voff, nlds + ks * 1024);
    }
}
DI u32x4 pair16(const f32x4& a, const f32x4& b) {
    auto x0 = __builtin_amdgcn_permlane16_swap(pk2(a[0], a[1]), pk2(b[0], b[1]), false, false);
    auto x1 = __builtin_amdgcn_permlane16_swap(pk2(a[2], a[3]), pk2(b[2], b[3]), false, false);
    return (u32x4){x0[0], x1[0], x0[1], x1[1]};
}
DI u32x4 pack8(const f32x4& a, const f32x4& b) { return (u32x4){pk2(a[0], a[1]), pk2(a[2], a[3]), pk2(b[0], b[1]), pk2(b[2], b[3])}; }


DI void qk_issue(const Ctx& p, unsigned la, int h, int w, int l, int ps) {
    const u16* src = ((ps >> 1) ? p.Wk : p.Wq) + h * 128 + (size_t)(32 * 9 * (ps & 1)) * 4096;
    const unsigned o = wblk_lane_off(4096, w, l, false), dst = la + ((ps & 1) ? 0 : XBUF) + w * 1024;
#pragma unroll
    for (int k = 0; k < 9; ++k) dma16(src + (size_t)(32 * k) * 4096, o, dst + k * 8192);
}
template <int PS>
DI void qk_consume(const char* lds, int w, int l, u32x4 (&wa)[18], u32x4 (&wb)[18]) {
    const char* b = lds + ((PS & 1) ? 0 : XBUF);
#pragma unroll
    for (int k = 0; k < 9; ++k) { u32x4 f = wblk_frag(b + k * 8192, w, l); if (PS >> 1) wb[9 * (PS & 1) + k] = f; else wa[9 * (PS & 1) + k] = f; }
}
DI void vu_issue(const Ctx& p, unsigned la, int h, int w, int l, int ps) {
    const unsigned dst = la + ((ps & 1) ? 0 : XBUF) + w * 1024;
    if (ps < 2) {
        const u16* src = p.Wv + h * 128 + (size_t)(32 * 9 * ps) * 4096;
        const unsigned o = wblk_lane_off(4096, w, l, false);
#pragma unroll
        for (int k = 0; k < 9; ++k) dma16(src + (size_t)(32 * k) * 4096, o, dst + k * 8192);
    } else {
        const u16* src = p.Wo + (size_t)h * 128 * 576;
        const unsigned o = wblk_lane_off(576, w, l, false);
        const int r = l >> 4, ch = (l & 15) ^ ((r << 2) | (w & 3));
        const unsigned o2 = (unsigned)((4 * w + r + 32 * (ch >> 3)) * 576 + 512 + (ch & 7) * 8) * 2u;
#pragma unroll
        for (int k = 0; k < 9; ++k) {
            const int b0 = k, b1 = 9 + k;
            if (ps == 2) dma16(src + (size_t)(32 * (b0 & 3)) * 576 + (b0 >> 2) * 128, o, dst + k * 8192);
            else if (b1 < 16) dma16(src + (size_t)(32 * (b1 & 3)) * 576 + (b1 >> 2) * 128, o, dst + k * 8192);
            else dma16(src + (size_t)(64 * (b1 - 16)) * 576, o2, dst + k * 8192);
        }
    }
}
template <int PS>
DI void vu_consume(const char* lds, int w, int l, u32x4 (&wv)[18], u32x4 (&wo)[5][4]) {
    const char* b = lds + ((PS & 1) ? 0 : XBUF);
#pragma unroll
    for (int k = 0; k < 9; ++k) {
        if (PS < 2) wv[9 * PS + k] = wblk_frag(b + k * 8192, w, l);
        else {
            const int bb = 9 * (PS - 2) + k;
            if (bb < 16) wo[bb >> 2][bb & 3] = wblk_frag(b + k * 8192, w, l);
            else { wo[4][2 * (bb - 16)] = wblk_frag(b + k * 8192, w & 3, l); wo[4][2 * (bb - 16) + 1] = wblk_frag(b + k * 8192, 4 + (w & 3), l); }
        }
    }
}
#define STREAM_W(ISSUE, CONSUME, A1, A2, pubcnt, xa, xb_) do { \
    ISSUE(p, la, h, w, l, 1); WAITV(9); bar(); if ((pubcnt) && threadIdx.x == 0) cnt_add(p, (pubcnt)); \
    CONSUME<0>(lds, w, l, A1, A2); bar(); \
    ISSUE(p, la, h, w, l, 2); WAITV(9); bar(); CONSUME<1>(lds, w, l, A1, A2); bar(); \
    ISSUE(p, la, h, w, l, 3); WAITV(9); bar(); CONSUME<2>(lds, w, l, A1, A2); bar(); \
    if ((xa) >= 0) { dma_x(p.ws, (xa), la + XBUF, w, l); WAITV(9); } else { WAITV(0); } \
    bar(); CONSUME<3>(lds, w, l, A1, A2); bar(); \
    if ((xb_) >= 0) dma_x(p.ws, (xb_), la, w, l); } while (0)

DI void worker_qk(const Ctx& p, rsrc_t rs, char* lds, int h, int par, unsigned fcnt) {
    const int w = __builtin_amdgcn_readfirstlane(threadIdx.x >> 6), l = tid_() & 63;
    const int n16 = l & 15, kq = l >> 4;
    u32x4 wa[18], wb[18];
    const unsigned la = lds_addr(lds);
    STREAM_W(qk_issue, qk_consume, wa, wb, fcnt, 0, 1 + par);
    const int ks4 = w >> 1, kqq = 2 * (w & 1) + (kq >> 1), half = kq & 1;
    const unsigned obase = ((unsigned)h * 8 + half * 4 + ks4) * 1024 + (n16 + 16 * kqq) * 16;
    {
        WAITV(9); bar();
        f32x4 acc[2][4];
        tile_gemm<2, false, 2>(lds + XBUF + par * 36 * 1024 + l * 16, wa, wb, acc);
        const unsigned off = obase + (unsigned)par * 32 * 8192;
        sts16(rs, rmap(OFF_QF + off, p.sel), pair16(acc[0][0], acc[0][1]));
        sts16(rs, rmap(OFF_KF + off, p.sel), pair16(acc[1][0], acc[1][1]));
        WAITV(0); bar();
        if (threadIdx.x == 0) cnt_add(p, C_K);
    }
    const int NTL = 16 - par;
#pragma unroll 1
    for (int n = 0; n < NTL; ++n) {
        const int T = 1 + par + 2 * n;
        if (n < 2) WAITV(0); else WAITV(4);
        bar();
        if (n >= 1 && threadIdx.x == 0) { if (n < 2) cnt_add(p, C_K + (T - 2) * 128); else if (n > 2) cnt_add(p, C_K + (T - 4) * 128); }
        f32x4 acc[2][4];
        tile_gemm<2, false>(lds + (n & 1) * XBUF + l * 16, wa, wb, acc, n + 1 < NTL ? p.ws + OFF_XF + (size_t)((T + 2) * 72 + w * 9) * 1024 : nullptr, la + ((n + 1) & 1) * XBUF + w * 9216, l * 16);
#pragma unroll
        for (int sg = 0; sg < 2; ++sg) {
            const unsigned off = obase + (unsigned)(2 * T + sg) * 32 * 8192;
            sts16(rs, rmap(OFF_QF + off, p.sel), pair16(acc[0][2 * sg], acc[0][2 * sg + 1]));
            sts16(rs, rmap(OFF_KF + off, p.sel), pair16(acc[1][2 * sg], acc[1][2 * sg + 1]));
        }
    }
    WAITV(0);
    bar();
    if (threadIdx.x < 2) cnt_add(p, C_K + (1 + par + 2 * (NTL - 2 + threadIdx.x)) * 128);
}

template <int NT>
DI void vu_tile(const Ctx& p, rsrc_t rs, char* xb, const char* xsrc, int seg0, int w, int l, const u32x4 (&wv)[18], const u32x4 (&wo)[5][4], unsigned ub, int nextT, unsigned nlds) {
    const int n16 = l & 15, kq = l >> 4;
    const int ks4 = w >> 1, kqq = 2 * (w & 1) + (kq >> 1), half = kq & 1;
    f32x4 acc[2][4];
    tile_gemm<1, false, NT>(xsrc + l * 16, wv, wv, acc);
    bar();
#pragma unroll
    for (int t = 0; t < NT; ++t)
        *(u32x2*)(xb + (t * 4 + ks4) * 1024 + (n16 + 16 * kqq) * 16 + half * 8) = (u32x2){pk2(acc[0][t][0], acc[0][t][1]), pk2(acc[0][t][2], acc[0][t][3])};
    bar();
    u32x4 vf[NT][4];
#pragma unroll
    for (int t = 0; t < NT; ++t)
#pragma unroll
        for (int ks = 0; ks < 4; ++ks) vf[t][ks] = *(const u32x4*)(xb + (t * 4 + ks) * 1024 + l * 16);
    bar();
    if (nextT >= 0) dma_x(p.ws, nextT, nlds, w, l);
#pragma unroll
    for (int i = 0; i < 5; ++i) {
        if (i < 4 || w < 4) {
            f32x4 u[NT];
#pragma unroll
            for (int t = 0; t < NT; ++t) u[t] = (f32x4){0, 0, 0, 0};
#pragma unroll
            for (int ks = 0; ks < 4; ++ks)
#pragma unroll
                for (int t = 0; t < NT; ++t) u[t] = mfma(vf[t][ks], wo[i][ks], u[t]);
#pragma unroll
            for (int sg = 0; sg < NT / 2; ++sg) sts16(rs, rmap(ub + ((unsigned)(seg0 + sg) * 32 * 36 + 8 * i) * 1024, p.sel ^ FARU), pair16(u[2 * sg], u[2 * sg + 1]));
        }
    }
}
DI void worker_vu(const Ctx& p, rsrc_t rs, char* lds, int h, int par, unsigned fcnt) {
    const int w = __builtin_amdgcn_readfirstlane(threadIdx.x >> 6), l = tid_() & 63;
    const int n16 = l & 15, kq = l >> 4;
    const unsigned la = lds_addr(lds);
    u32x4 wv[18], wo[5][4];
    STREAM_W(vu_issue, vu_consume, wv, wo, fcnt, 0, 1 + par);
    const unsigned ub = ((unsigned)h * 36 + w) * 1024 + (perm16(n16) + 16 * (2 * (kq & 1) + (kq >> 1))) * 16;
    const int NTL = 16 - par;
    WAITV(9); bar();
    vu_tile<2>(p, rs, lds + XBUF, lds + XBUF + par * 36 * 1024, par, w, l, wv, wo, ub, 3 + par, la + XBUF);
    WAITV(0); bar();
    if (threadIdx.x == 0) cnt_add(p, C_U);
#pragma unroll 1
    for (int n = 0; n < NTL; ++n) {
        const int T = 1 + par + 2 * n;
        char* xb = lds + (n & 1) * XBUF;
        if (n < 2) WAITV(0); else WAITV(17);
        bar();
        if (n >= 1 && threadIdx.x == 0) { if (n < 2) cnt_add(p, C_U + (T - 2) * 128); else if (n > 2) cnt_add(p, C_U + (T - 4) * 128); }
        vu_tile<4>(p, rs, xb, xb, 2 * T, w, l, wv, wo, ub, n + 2 < NTL ? T + 4 : -1, la + (n & 1) * XBUF);
        if (n == NTL - 2) { WAITV(8); }
    }
    WAITV(0);
    bar();
    if (threadIdx.x < 2) cnt_add(p, C_U + (1 + par + 2 * (NTL - 2 + threadIdx.x)) * 128);
}

template <bool VPART>
DI void bgemm_body(const Ctx& p, rsrc_t rs, char* lds, int cg, int tg) {
    const int w = __builtin_amdgcn_readfirstlane(threadIdx.x >> 6), l = tid_() & 63;
    const int n16 = l & 15, kq = l >> 4;
    u32x4 wa[18], wb[18];
    load_w2(p.bw + cg * 192, p.bw + cg * 192 + 128, 1728, true, lds, w, l, wa, wb);
    const unsigned la = lds_addr(lds);
    dma_x(p.ws, tg * 4, la, w, l);
#pragma unroll 1
    for (int n = 0; n < 4; ++n) {
        const int T = tg * 4 + n;
        if (n == 0) WAITV(0); else WAITV(2);
        bar();
        f32x4 acc[2][4];
        const char* nsrc = n + 1 < 4 ? p.ws + OFF_XF + (size_t)((T + 1) * 72 + w * 9) * 1024 : nullptr;
        const unsigned nlds = la + ((n + 1) & 1) * XBUF + w * 9216;
        if (w < 4) tile_gemm<2, VPART>(lds + (n & 1) * XBUF + l * 16, wa, wb, acc, nsrc, nlds, l * 16);
        else tile_gemm<1, VPART>(lds + (n & 1) * XBUF + l * 16, wa, wb, acc, nsrc, nlds, l * 16);
#pragma unroll
        for (int i = 0; i < 2; ++i) {
            if (i == 0 || w < 4) {
                const int nt = cg * 12 + 8 * i + w;
                if (!VPART) {
#pragma unroll
                    for (int sg = 0; sg < 2; ++sg)
                        sts16(rs, OFF_QB + (unsigned)(T * 64 + (2 * sg + (kq & 1)) * 16 + n16) * 2304 + (16 * nt + 8 * (kq >> 1)) * 2, pair16(acc[i][2 * sg], acc[i][2 * sg + 1]));
                } else {
                    const int m = 16 * nt + n16 - 1152, h = m / 18, d = m - h * 18;
#pragma unroll
                    for (int b = 0; b < 2; ++b)
                        sts16(rs, OFF_VB + (((unsigned)h * 64 + T * 2 + b) * 2 + (d >> 4)) * 1024 + ((d & 15) + 16 * kq) * 16, pack8(acc[i][2 * b], acc[i][2 * b + 1]));
                }
            }
        }
    }
    wg_publish(p, C_BG + tg * 128);
}

DI void bgemm_task(const Ctx& p, rsrc_t rs, char* lds, int t) {
    const int cg = t % 9, tg = t / 9;
    if (cg < 6) bgemm_body<false>(p, rs, lds, cg, tg); else bgemm_body<true>(p, rs, lds, cg, tg);
}

DI u32x4 maskd(u32x4 v, int kq) {
    if (kq >= 2) { v.y = 0; v.z = 0; v.w = 0; }
    if (kq == 3) v.x = 0;
    return v;
}
DI void task_battn(const Ctx& p, rsrc_t rs, char* lds, int qg, int h) {
    const int w = __builtin_amdgcn_readfirstlane(threadIdx.x >> 6), l = tid_() & 63;
    const int n16 = l & 15, kq = l >> 4;
    if (qg) cnt_wait(p, C_BG + (qg - 1) * 128, 9);
    cnt_wait(p, C_BG + qg * 128, 9);
    const int kqc = kq < 3 ? kq : 2;
    const int b0 = qg * 8 + w;
    {
        u32x4 kv[2][4];
#pragma unroll
        for (int e = 0; e < 2; ++e) {
            int kb = 8 * qg - 8 + 2 * w + e; if (kb < 0) kb = 0;
#pragma unroll
            for (int kt = 0; kt < 2; ++kt) kv[e][kt] = lds16(rs, OFF_QB + (unsigned)(32 * kb + 16 * kt + n16) * 2304 + 1152 + h * 36 + kqc * 16);
            kv[e][2] = lds16(rs, OFF_VB + (((unsigned)h * 64 + kb) * 2) * 1024 + l * 16);
            kv[e][3] = lds16(rs, OFF_VB + (((unsigned)h * 64 + kb) * 2 + 1) * 1024 + ((n16 & 1) + 16 * kq) * 16);
        }
#pragma unroll
        for (int e = 0; e < 2; ++e) {
            *(u32x4*)(lds + ((2 * w + e) * 4 + 0) * 1024 + l * 16) = maskd(kv[e][0], kq);
            *(u32x4*)(lds + ((2 * w + e) * 4 + 1) * 1024 + l * 16) = maskd(kv[e][1], kq);
            *(u32x4*)(lds + ((2 * w + e) * 4 + 2) * 1024 + l * 16) = kv[e][2];
            *(u32x4*)(lds + ((2 * w + e) * 4 + 3) * 1024 + l * 16) = kv[e][3];
        }
    }
    u32x4 qf[2];
#pragma unroll
    for (int qt = 0; qt < 2; ++qt) qf[qt] = maskd(lds16(rs, OFF_QB + (unsigned)(32 * b0 + 16 * qt + n16) * 2304 + h * 36 + kqc * 16), kq);
    bar();
    const char* kvb = lds + w * 4096 + l * 16;
    f32x4 s[2][9][2];
    const float scale = 0.23570226039551584f;
    const int nfirst = 8 - b0;
    const int dq = n16 - 4 * kq;
#pragma unroll
    for (int x = 0; x < 9; ++x) {
#pragma unroll
        for (int kt = 0; kt < 2; ++kt) {
            u32x4 kf = *(const u32x4*)(kvb + (x * 4 + kt) * 1024);
#pragma unroll
            for (int qt = 0; qt < 2; ++qt) {
                if ((x == 0 && qt == 1 && kt == 0) || (x == 8 && qt == 0 && kt == 1)) { s[qt][x][kt] = (f32x4){-INFINITY, -INFINITY, -INFINITY, -INFINITY}; continue; }
                f32x4 a = mfma(kf, qf[qt], (f32x4){0, 0, 0, 0});
                unsigned p0 = pk2(a[0], a[1]), p1 = pk2(a[2], a[3]);
                f32x4 b = {__uint_as_float(p0 << 16), __uint_as_float(p0 & 0xffff0000u), __uint_as_float(p1 << 16), __uint_as_float(p1 & 0xffff0000u)};
                b *= scale;
                p0 = pk2(b[0], b[1]); p1 = pk2(b[2], b[3]);
                a = (f32x4){__uint_as_float(p0 << 16), __uint_as_float(p0 & 0xffff0000u), __uint_as_float(p1 << 16), __uint_as_float(p1 & 0xffff0000u)};
                if (x == 0 && qt == kt) {
#pragma unroll
                    for (int r = 0; r < 4; ++r) if (dq - r >= 0) a[r] = -INFINITY;
                }
                if (x == 8 && qt == kt) {
#pragma unroll
                    for (int r = 0; r < 4; ++r) if (dq - r < 0) a[r] = -INFINITY;
                }
                if (x < 8 && x < nfirst) a = (f32x4){-INFINITY, -INFINITY, -INFINITY, -INFINITY};
                s[qt][x][kt] = a;
            }
        }
    }
    u32x4 pf[2][9];
#pragma unroll
    for (int qt = 0; qt < 2; ++qt) {
        float m = -INFINITY;
#pragma unroll
        for (int x = 0; x < 9; ++x)
#pragma unroll
            for (int kt = 0; kt < 2; ++kt) m = max3(max3(m, s[qt][x][kt][0], s[qt][x][kt][1]), s[qt][x][kt][2], s[qt][x][kt][3]);
        m = xmax4(m);
        const float L2E = 1.4426950408889634f;
        f32x4 sum4 = {0, 0, 0, 0};
#pragma unroll
        for (int x = 0; x < 9; ++x)
#pragma unroll
            for (int kt = 0; kt < 2; ++kt) {
                f32x4 d = (s[qt][x][kt] - m) * L2E;
#pragma unroll
                for (int r = 0; r < 4; ++r) s[qt][x][kt][r] = __builtin_amdgcn_exp2f(d[r]);
                sum4 += s[qt][x][kt];
            }
        float sum = xsum4((sum4[0] + sum4[1]) + (sum4[2] + sum4[3]));
        const float inv = 1.0f / sum;
#pragma unroll
        for (int x = 0; x < 9; ++x) pf[qt][x] = pack8(s[qt][x][0] * inv, s[qt][x][1] * inv);
    }
    f32x4 o[2][2];
#pragma unroll
    for (int qt = 0; qt < 2; ++qt) { o[qt][0] = (f32x4){0, 0, 0, 0}; o[qt][1] = (f32x4){0, 0, 0, 0}; }
#pragma unroll
    for (int x = 0; x < 9; ++x) {
        u32x4 v0 = *(const u32x4*)(kvb + (x * 4 + 2) * 1024);
        u32x4 v1 = *(const u32x4*)(kvb + (x * 4 + 3) * 1024);
#pragma unroll
        for (int qt = 0; qt < 2; ++qt) { o[qt][0] = mfma(v0, pf[qt][x], o[qt][0]); o[qt][1] = mfma(v1, pf[qt][x], o[qt][1]); }
    }
#pragma unroll
    for (int qt = 0; qt < 2; ++qt) {
        const unsigned row = OFF_OB + (unsigned)(32 * b0 + 16 * qt + n16) * 1152 + h * 36;
        sts4(rs, row + kq * 8, pk2(o[qt][0][0], o[qt][0][1]));
        sts4(rs, row + kq * 8 + 4, pk2(o[qt][0][2], o[qt][0][3]));
        if (kq == 0) sts4(rs, row + 32, pk2(o[qt][1][0], o[qt][1][1]));
    }
    wg_publish(p, C_BA + qg * 128);
}

DI void task_bout(const Ctx& p, rsrc_t rs, char* lds, int cg, int T) {
    const int w = __builtin_amdgcn_readfirstlane(threadIdx.x >> 6), l = tid_() & 63;
    const int n16 = l & 15, kq = l >> 4;
    u32x4 wa[18], wb[18];
    load_w2(p.bo + cg * 192, p.bo + cg * 192 + 128, 576, true, lds, w, l, wa, wb);
    cnt_wait(p, C_BA + (T >> 2) * 128, 32);
    {
        u32x4 v[9];
#pragma unroll
        for (int i = 0; i < 9; ++i) { const int f = w * 9 + i, tt = f / 18, ks = f % 18; v[i] = lds16(rs, OFF_OB + (unsigned)(T * 64 + tt * 16 + n16) * 1152 + ks * 64 + kq * 16); }
#pragma unroll
        for (int i = 0; i < 9; ++i) *(u32x4*)(lds + (w * 9 + i) * 1024 + l * 16) = v[i];
    }
    bar();
    f32x4 acc[2][4];
    if (w < 4) tile_gemm<2, false>(lds + l * 16, wa, wb, acc);
    else tile_gemm<1, false>(lds + l * 16, wa, wb, acc);
#pragma unroll
    for (int i = 0; i < 2; ++i) {
        if (i == 0 || w < 4) {
            const int nt = cg * 12 + 8 * i + w;
#pragma unroll
            for (int sg = 0; sg < 2; ++sg)
                sts16(rs, OFF_BB + (unsigned)(T * 64 + (2 * sg + (kq & 1)) * 16 + n16) * 1152 + (16 * nt + 8 * (kq >> 1)) * 2, pair16(acc[i][2 * sg], acc[i][2 * sg + 1]));
        }
    }
    wg_publish(p, C_BB + T * 128);
}

DI bool wave_ok(u32x4 v) {
    unsigned m = max(max(v.x, v.y), max(v.z, v.w));
    return __builtin_amdgcn_ballot_w64(m == 0xFFFFFFFFu) == 0ull;
}
DI u32x4 poll1(const char* a, int& spins) {
    u32x4 v;
    for (spins = 0;; ++spins) {
        asm volatile("global_load_dwordx4 %0, %1, off sc1\n\ts_waitcnt vmcnt(0)" : "=&v"(v) : "v"(a) : "memory");
        if (wave_ok(v) || spins > SPIN_CAP) break;
    }
    return v;
}
DI void dly_wait(unsigned long long ref, int D) { while ((int)(unsigned)(__builtin_amdgcn_s_memtime() - ref) < D) __builtin_amdgcn_s_sleep(1); }
DI int dly_upd(int D, int spins, int lo, int hi) {
    D += spins == 0 ? -ADN : (spins == 1 ? AUP : AUP2);
    return min(max(D, lo), hi);
}
DI u32x4 umax4(u32x4 a, u32x4 b) { return (u32x4){max(a.x, b.x), max(a.y, b.y), max(a.z, b.z), max(a.w, b.w)}; }
template <int N>
DI void polln(const char* const (&a)[N], u32x4 (&v)[N], int& spins) {
    for (spins = 0;; ++spins) {
        u32x4 m;
        if constexpr (N == 2) {
            asm volatile("global_load_dwordx4 %0, %2, off sc1\n\tglobal_load_dwordx4 %1, %3, off sc1\n\ts_waitcnt vmcnt(0)"
                         : "=&v"(v[0]), "=&v"(v[1]) : "v"(a[0]), "v"(a[1]) : "memory");
            m = umax4(v[0], v[1]);
        } else if constexpr (N == 3) {
            asm volatile("global_load_dwordx4 %0, %3, off sc1\n\tglobal_load_dwordx4 %1, %4, off sc1\n\tglobal_load_dwordx4 %2, %5, off sc1\n\ts_waitcnt vmcnt(0)"
                         : "=&v"(v[0]), "=&v"(v[1]), "=&v"(v[2]) : "v"(a[0]), "v"(a[1]), "v"(a[2]) : "memory");
            m = umax4(umax4(v[0], v[1]), v[2]);
        } else {
            asm volatile("global_load_dwordx4 %0, %4, off sc1\n\tglobal_load_dwordx4 %1, %5, off sc1\n\tglobal_load_dwordx4 %2, %6, off sc1\n\tglobal_load_dwordx4 %3, %7, off sc1\n\ts_waitcnt vmcnt(0)"
                         : "=&v"(v[0]), "=&v"(v[1]), "=&v"(v[2]), "=&v"(v[3]) : "v"(a[0]), "v"(a[1]), "v"(a[2]), "v"(a[3]) : "memory");
            m = umax4(umax4(v[0], v[1]), umax4(v[2], v[3]));
        }
        if (wave_ok(m) || spins > SPIN_CAP) break;
    }
}

constexpr int LA_MEM = 0;
constexpr int LA_QK = 9216;
constexpr int LA_KX = 16384;
constexpr int LA_QX = 40960;
constexpr int LA_X = 13312;
constexpr int LA_PT = 65536;

DI f32x4 rscore(f32x4 a) {
    unsigned p0 = pk2(a[0], a[1]), p1 = pk2(a[2], a[3]);
    f32x4 b = {__uint_as_float(p0 << 16), __uint_as_float(p0 & 0xffff0000u), __uint_as_float(p1 << 16), __uint_as_float(p1 & 0xffff0000u)};
    b *= 0.08838834764831845f;
    p0 = pk2(b[0], b[1]); p1 = pk2(b[2], b[3]);
    f32x4 c = {__uint_as_float(p0 << 16), __uint_as_float(p0 & 0xffff0000u), __uint_as_float(p1 << 16), __uint_as_float(p1 & 0xffff0000u)};
    return c * 1.4426950408889634f;
}
DI void softmax12(f32x4& s0, f32x4& s1, f32x4& s2, u32x4& px, u32x2& pm) {
    s0 = rscore(s0); s1 = rscore(s1); s2 = rscore(s2);
    asm volatile("" : "+v"(s0), "+v"(s1), "+v"(s2));
    float m = max3(max3(max3(s0[0], s0[1], s0[2]), s0[3], s1[0]), max3(s1[1], s1[2], s1[3]), max3(max3(s2[0], s2[1], s2[2]), s2[3], s2[3]));
    m = xmax4(m);
    s0 -= m; s1 -= m; s2 -= m;
#pragma unroll
    for (int r = 0; r < 4; ++r) {
        s1[r] = __builtin_amdgcn_exp2f(s1[r]);
        s2[r] = __builtin_amdgcn_exp2f(s2[r]);
        s0[r] = __builtin_amdgcn_exp2f(s0[r]);
    }
    f32x4 t = s0 + s1 + s2;
    float sum = xsum4((t[0] + t[1]) + (t[2] + t[3]));
    const float inv = __builtin_amdgcn_rcpf(sum);
    s0 *= inv; s1 *= inv; s2 *= inv;
    px = pair16(s1, s2);
    pm = (u32x2){pk2(s0[0], s0[1]), pk2(s0[2], s0[3])};
}

DI int pgrp(int s) { return s >= 56 ? s : (s | (PGRP - 1)); }
DI void role_A(const Ctx& p, rsrc_t rs, char* lds, int h) {
    const int w = __builtin_amdgcn_readfirstlane(threadIdx.x >> 6), l = tid_() & 63;
    const int n16 = l & 15, kq = l >> 4;
    const unsigned selmask = load_selmask(p.ws);
    const unsigned selM = selmask & 1u, selP0 = (selmask >> 2) & 1u, selP1 = selP0;
    u32x4 wq[18], wk[18];
    {
        const unsigned la = lds_addr(lds);
        qk_issue(p, la, h, w, l, 0);
        STREAM_W(qk_issue, qk_consume, wq, wk, 0u, -1, -1);
    }
    const int idx = (l & 8) ? 0xEEEE : 0x4444;
    const unsigned moff = amap(w * 1024 + l * 16, selM), moff8 = amap(8 * 1024 + l * 16, selM);
    auto prefetch = [&](int sn) {
        const int l2 = tid_() & 63;
        const unsigned src = rmap(((w & 1) ? OFF_KF : OFF_QF) + (unsigned)(sn * 32 + h) * 8192, p.sel) + l2 * 16;
        char* dst = lds + ((w & 1) ? LA_KX : LA_QX) + (sn % 3) * 8192 + l2 * 16;
        u32x4 st[8];
#pragma unroll
        for (int i = 0; i < 8; ++i) st[i] = lds16(rs, src + i * 1024);
#pragma unroll
        for (int i = 0; i < 8; ++i) *(u32x4*)(dst + i * 1024) = st[i];
    };
    if (w >= 3 && w < 7) { cnt_wait(p, C_K, 64); prefetch(w >= 5 ? 0 : 1); }
    __builtin_amdgcn_s_waitcnt(0);
    bar();
    const float NI = -INFINITY;
    const float z0 = kq < 2 ? NI : 0.f;
    const f32x4 i0 = {z0, z0, z0, z0};
    auto pt_store = [&](int sp) {
        const int l2 = tid_() & 63, k2 = l2 >> 4;
#pragma unroll
        for (int tw = 0; tw < 2; ++tw) {
            u32x4 a = *(const u32x4*)(lds + LA_PT + (sp & 1) * 4096 + tw * 2048 + l2 * 16);
            u32x2 b = *(const u32x2*)(lds + LA_PT + (sp & 1) * 4096 + tw * 2048 + 1024 + l2 * 8);
            const unsigned pt = OFF_PT + (unsigned)(sp * 32 + h) * 2560 + (16 * tw + (l2 & 15)) * 16;
            sts16(rs, rmap(pt + (1 + 2 * (k2 & 1) + (k2 >> 1)) * 512, p.sel ^ PSIDE), a);
            if (k2 >= 2) sts8(rs, rmap(pt + (k2 - 2) * 8, p.sel ^ PSIDE), b);
        }
    };
    const unsigned pox = amap(h * 640 + (1 + 2 * (kq & 1) + (kq >> 1)) * 128 + (n16 & 7) * 16, 0), pom = amap(h * 640 + (n16 & 7) * 16, 0);
    const int qkw = (16 * w + n16) * 32 + (l >> 5) * 16 + ((l >> 4) & 1) * 8;
    const int trb = LA_QK + (8 * kq + (n16 >> 2)) * 32 + (l & 3) * 8;
    unsigned long long tq = __builtin_amdgcn_s_memtime() - 1000000;
    int DA = A0;
#pragma unroll 1
    for (int s = 0; s < NSEG; ++s) {
        u32x4 kc[8];
        if (w == 0) {
#pragma unroll
            for (int i = 0; i < 8; ++i) kc[i] = *(const u32x4*)(lds + LA_KX + (s % 3) * 8192 + i * 1024 + l * 16);
        }
        const unsigned psb = p_slot(s) + (((s >> 5) ? selP1 : selP0) << 13);
        dly_wait(tq, DA);
        {
            int np;
            const char* ms = p.ws + mem_slot(0, s);
            if (w == 7) { const char* aa[2] = {ms + moff, ms + moff8}; u32x4 v[2]; polln<2>(aa, v, np); *(u32x4*)(lds + LA_MEM + 7 * 1024 + l * 16) = v[0]; *(u32x4*)(lds + LA_MEM + 8 * 1024 + l * 16) = v[1]; }
            else { u32x4 a = poll1(ms + moff, np); *(u32x4*)(lds + LA_MEM + w * 1024 + l * 16) = a; }
            if (s > 0) DA = dly_upd(DA, np, 0, AHI);
        }
        bar();
        u32x4 af[9];
#pragma unroll
        for (int k2 = 0; k2 < 9; ++k2) af[k2] = *(const u32x4*)(lds + LA_MEM + k2 * 1024 + l * 16);
        SCHED();
        f32x4 aq = {0, 0, 0, 0}, ak = {0, 0, 0, 0};
#pragma unroll
        for (int k2 = 0; k2 < 9; ++k2) {
            aq = smfmac(af[k2], wq[2 * k2], wq[2 * k2 + 1], aq, idx);
            ak = smfmac(af[k2], wk[2 * k2], wk[2 * k2 + 1], ak, idx);
        }
        {
            float v[4];
#pragma unroll
            for (int r = 0; r < 4; ++r) {
                auto sw = __builtin_amdgcn_permlane32_swap(__float_as_uint(aq[r]), __float_as_uint(ak[r]), false, false);
                v[r] = __uint_as_float(sw[0]) + __uint_as_float(sw[1]);
            }
            u32x2 o = {pk2(v[0], v[1]), pk2(v[2], v[3])};
            *(u32x2*)(lds + LA_QK + qkw) = o;
        }
        bar();
        tq = __builtin_amdgcn_s_memtime();
        if (w == 0) {
            if (s < NSEG - 1) {
                __builtin_amdgcn_s_setprio(3);
                u32x4 F[4];
#pragma unroll
                for (int ks = 0; ks < 4; ++ks) {
                    u32x2 a = trread(lds + trb + ks * 1024);
                    u32x2 b = trread(lds + trb + ks * 1024 + 128);
                    F[ks] = (u32x4){a.x, a.y, b.x, b.y};
                }
                SCHED();
                const float cs = 0.08838834764831845f * 1.4426950408889634f;
                f32x4 s0 = i0, s1 = {0, 0, 0, 0}, s2 = {0, 0, 0, 0};
#pragma unroll
                for (int ks = 0; ks < 4; ++ks) { s1 = mfma(kc[ks], F[ks], s1); s2 = mfma(kc[4 + ks], F[ks], s2); s0 = mfma(F[ks], F[ks], s0); SCHED(); }
                const f32x4 r0 = s0, r1 = s1, r2 = s2;
                s1 *= cs; s2 *= cs; s0 *= cs;
                const float ml = max3(max3(max3(s1[0], s1[1], s1[2]), s1[3], s2[0]), max3(s2[1], s2[2], s2[3]), max3(max3(s0[0], s0[1], s0[2]), s0[3], s0[3]));
                if (__builtin_amdgcn_ballot_w64(!(fabsf(ml) <= GUARD) && n16 < 8) != 0ull) {
                    s0 = rscore(r0); s1 = rscore(r1); s2 = rscore(r2);
                    asm volatile("" : "+v"(s0), "+v"(s1), "+v"(s2));
                    const float m = xmax4(max3(max3(max3(s1[0], s1[1], s1[2]), s1[3], s2[0]), max3(s2[1], s2[2], s2[3]), max3(max3(s0[0], s0[1], s0[2]), s0[3], s0[3])));
                    s0 -= m; s1 -= m; s2 -= m;
                }
#pragma unroll
                for (int r = 0; r < 4; ++r) { s1[r] = __builtin_amdgcn_exp2f(s1[r]); s2[r] = __builtin_amdgcn_exp2f(s2[r]); s0[r] = __builtin_amdgcn_exp2f(s0[r]); }
                f32x4 t = (s1 + s2) + s0;
                const float inv = __builtin_amdgcn_rcpf(xsum4((t[0] + t[1]) + (t[2] + t[3])));
                s1 *= inv; s2 *= inv; s0 *= inv;
                u32x4 px = pair16(s1, s2);
                if (n16 < 8) sts16(rs, psb + pox, (u32x4){nos(px.x), nos(px.y), nos(px.z), nos(px.w)});
                u32x4 pm = pair16(s0, s0);
                if (n16 < 8 && kq == 2) sts16(rs, psb + pom, (u32x4){nos(pm.x), nos(pm.y), nos(pm.z), nos(pm.w)});
                __builtin_amdgcn_s_setprio(0);
            }
        } else if (w < 3) {
            const int tw = w - 1;
            const int l2 = tid_() & 63, n2 = l2 & 15, k2 = l2 >> 4;
            const int trb2 = LA_QK + (8 * k2 + (n2 >> 2)) * 32 + (l2 & 3) * 8;
            u32x4 F[4], Bq[4], kk[8];
#pragma unroll
            for (int ks = 0; ks < 4; ++ks) {
                u32x2 a = trread(lds + trb2 + ks * 1024);
                u32x2 b = trread(lds + trb2 + ks * 1024 + 128);
                F[ks] = (u32x4){a.x, a.y, b.x, b.y};
                Bq[ks] = *(const u32x4*)(lds + LA_QX + (s % 3) * 8192 + (tw * 4 + ks) * 1024 + l2 * 16);
                kk[ks] = *(const u32x4*)(lds + LA_KX + (s % 3) * 8192 + ks * 1024 + l2 * 16);
                kk[4 + ks] = *(const u32x4*)(lds + LA_KX + (s % 3) * 8192 + (4 + ks) * 1024 + l2 * 16);
            }
            const int lim = 16 * tw + n2 - 4 * k2;
            f32x4 s0, s1, s2;
#pragma unroll
            for (int r = 0; r < 4; ++r) { s0[r] = k2 < 2 ? NI : 0.f; s1[r] = (r > lim) ? NI : 0.f; s2[r] = (16 + r > lim) ? NI : 0.f; }
#pragma unroll
            for (int ks = 0; ks < 4; ++ks) { s1 = mfma(kk[ks], Bq[ks], s1); s2 = mfma(kk[4 + ks], Bq[ks], s2); s0 = mfma(F[ks], Bq[ks], s0); }
            u32x4 hpx; u32x2 hpm;
            softmax12(s0, s1, s2, hpx, hpm);
            if (s >= NSEG - NTAIL) {
                const unsigned pt = OFF_PT + (unsigned)(s * 32 + h) * 2560 + (16 * tw + n2) * 16;
                sts16(rs, rmap(pt + (1 + 2 * (k2 & 1) + (k2 >> 1)) * 512, p.sel ^ PSIDE), (u32x4){nos(hpx.x), nos(hpx.y), nos(hpx.z), nos(hpx.w)});
                if (k2 >= 2) sts8(rs, rmap(pt + (k2 - 2) * 8, p.sel ^ PSIDE), (u32x2){nos(hpm.x), nos(hpm.y)});
            } else {
                *(u32x4*)(lds + LA_PT + (s & 1) * 4096 + tw * 2048 + l2 * 16) = hpx;
                *(u32x2*)(lds + LA_PT + (s & 1) * 4096 + tw * 2048 + 1024 + l2 * 8) = hpm;
            }
        } else if (w < 5) {
        } else if (w < 7) {
            if (s + 2 < NSEG) {
                if (!(s & 1)) cnt_wait(p, C_K + ((s + 2) >> 1) * 128, 32);
                prefetch(s + 2);
            }
        } else {
            const int l2 = tid_() & 63;
            if (s > 1 && s - 2 < NSEG - NTAIL && l2 == 0 && pgrp(s - 2) == s - 2) cnt_add(p, C_P + (s - 2) * 128);
            if (s > 0 && s - 1 < NSEG - NTAIL) pt_store(s - 1);
            unsigned ones = 0xFFFFFFFFu; asm volatile("" : "+v"(ones));
            if (s + PF < NSEG - 1 && l2 < 40) sts16(rs, p_slot(s + PF) + amap(h * 640 + l2 * 16, ((s + PF) >> 5) ? selP1 : selP0), (u32x4){ones, ones, ones, ones});
        }
    }
}

template <int NCT, int NKS>
DI void fold_unit(const Ctx& p, rsrc_t rs, char* sm, int h, int ct0, int ks0, unsigned cnt) {
    constexpr int LD = NCT * 32 + 16, CH = NCT * 2, TOT = 128 * CH, PER = (TOT + 511) / 512;
    const int t = tid_();
    const int w = __builtin_amdgcn_readfirstlane(threadIdx.x >> 6), l = t & 63;
    const int n16 = l & 15, kq = l >> 4;
    u32x4 v[PER];
#pragma unroll
    for (int i = 0; i < PER; ++i) { int c = t + 512 * i; if (c < TOT) { int r = c / CH, cc = c % CH; v[i] = *(const u32x4*)(p.Wo + (size_t)(h * 128 + r) * 576 + ct0 * 16 + cc * 8); } }
    constexpr int NR = (NKS + 7) / 8;
    const int npair = (NKS - w + 7) / 8;
    const u16* av = p.Wv + (size_t)(32 * (ks0 + w) + n16) * 4096 + h * 128 + 8 * kq;
    u32x4 a[NR][2][4];
#pragma unroll
    for (int m = 0; m < NR; ++m)
#pragma unroll
        for (int e = 0; e < 2; ++e)
#pragma unroll
            for (int ks = 0; ks < 4; ++ks) if (m < npair) a[m][e][ks] = *(const u32x4*)(av + (size_t)(m * 256 + e * 16) * 4096 + 32 * ks);
#pragma unroll
    for (int i = 0; i < PER; ++i) { int c = t + 512 * i; if (c < TOT) { int r = c / CH, cc = c % CH; *(u32x4*)(sm + r * LD + cc * 16) = v[i]; } }
    bar();
    const int q = n16 >> 2, pp = l & 3;
    const unsigned ob = OFF_WVOF + (((unsigned)h * 18 + ks0 + w) * 36 + ct0) * 1024 + (perm16(n16) + 16 * (2 * (kq & 1) + (kq >> 1))) * 16;
#pragma unroll
    for (int i = 0; i < NCT; ++i) {
        u32x4 bfr[4];
#pragma unroll
        for (int ks = 0; ks < 4; ++ks) {
            u32x2 x0 = trread(sm + (32 * ks + 8 * kq + q) * LD + i * 32 + pp * 8);
            u32x2 x1 = trread(sm + (32 * ks + 8 * kq + 4 + q) * LD + i * 32 + pp * 8);
            bfr[ks] = (u32x4){x0.x, x0.y, x1.x, x1.y};
        }
#pragma unroll
        for (int m = 0; m < NR; ++m) {
            if (m < npair) {
                f32x4 a0 = {0, 0, 0, 0}, a1 = {0, 0, 0, 0};
#pragma unroll
                for (int ks = 0; ks < 4; ++ks) { a0 = mfma(a[m][0][ks], bfr[ks], a0); a1 = mfma(a[m][1][ks], bfr[ks], a1); }
                sts16(rs, ob + (unsigned)(8 * m * 36 + i) * 1024, pair16(a0, a1));
            }
        }
    }
    if (cnt) wg_publish(p, cnt); else bar();
}

constexpr int LB_MEM = 0;
constexpr int LB_T = 9216;
constexpr int LB_PART = 13312;
DI void role_B(const Ctx& p, rsrc_t rs, char* lds, int j) {
    const int w = __builtin_amdgcn_readfirstlane(threadIdx.x >> 6), l = tid_() & 63;
    const int n16 = l & 15, kq = l >> 4;
    const unsigned selmask = load_selmask(p.ws);
    const unsigned selA = selmask & 1u, selM = ((selmask >> 1) & 1u) ^ MBSIDE, selP0 = (selmask >> 2) & 1u, selP1 = selP0;
    u32x4 w0[18], w1[18];
    if (WFK > 0) cnt_wait(p, C_F + (j / 18) * 128, 32);
    cnt_wait(p, C_F2 + (j / 24) * 128, 32);
    {
        const unsigned a = OFF_WVOF + ((unsigned)(4 * w + ((l >> 3) & 1)) * 18 * 36 + (j >> 1)) * 1024 + ((j & 1) * 8 + (l & 7) + 16 * kq) * 16;
#pragma unroll
        for (int i = 0; i < 18; ++i) { w0[i] = lds16(rs, a + i * 36 * 1024); w1[i] = lds16(rs, a + (36 + i) * 36 * 1024); }
    }
    const int idx = (l & 8) ? 0xEEEE : 0x4444;
    const unsigned moff = amap(w * 1024 + l * 16, selM), moff8 = amap(8 * 1024 + l * 16, selM);
    const int row = l & 7, bb = (l >> 3) & 1;
    const int G = j >> 1, ph = j & 1;
    const int up0 = w < 4 ? w + 4 : w - 4, up1 = w < 4 ? w + 12 : w + 4;
    const unsigned lo_m = (8 * (w & 3) + 4 * bb + kq) * 640 + row * 16;
    const unsigned lo_0 = (2 * up0 + bb) * 640 + (1 + kq) * 128 + row * 16;
    const unsigned lo_1 = (2 * up1 + bb) * 640 + (1 + kq) * 128 + row * 16;
    const unsigned soff = amap(j * 128 + row * 16, kq ? selM : selA) + (kq ? MB2 : 0u);
    const unsigned sofA = amap(j * 128 + (l >> 3) * 16, selA), sofB = amap(j * 128 + (l >> 3) * 16, selM) + MB2;
    const int pwo = LB_PART + ((4 * (kq & 1)) * 8 + row) * 80 + (w * 2 + bb) * 4;
    const int thead = 4 * w + 2 * (l >> 5) + (n16 >> 3);
    const int toff = (thead * 8 + (l & 7)) * 16 + ((l >> 4) & 1) * 8;
    const unsigned tgo = OFF_TF + ((unsigned)G * 32 + thead) * 256 + (ph * 8 + (l & 7)) * 16 + ((l >> 4) & 1) * 8;
    const unsigned ubase = ((unsigned)bb * 36 + G) * 1024 + (ph * 8 + row + 16 * kq) * 16;
    __builtin_amdgcn_s_waitcnt(0);
    u32x4 uf0 = {0, 0, 0, 0}, uf1 = {0, 0, 0, 0};
    int DB = B0, DM = M0;
    unsigned long long tb3 = __builtin_amdgcn_s_memtime() - 1000000;
#pragma unroll 1
    for (int s = 0; s < NSEG; ++s) {
        dly_wait(tb3, DM);
        {
            const char* ms = p.ws + mem_slot(1, s);
            int np;
            if (w == 7) { const char* aa[2] = {ms + moff, ms + moff8}; u32x4 v[2]; polln<2>(aa, v, np); *(u32x4*)(lds + LB_MEM + 7 * 1024 + l * 16) = v[0]; *(u32x4*)(lds + LB_MEM + 8 * 1024 + l * 16) = v[1]; }
            else { u32x4 a = poll1(ms + moff, np); *(u32x4*)(lds + LB_MEM + w * 1024 + l * 16) = a; }
            if (s > 0) DM = dly_upd(DM, np, 0, MHI);
        }
        bar();
        const unsigned long long tb1 = __builtin_amdgcn_s_memtime();
        u32x4 un0 = {0, 0, 0, 0}, un1 = {0, 0, 0, 0};
        if (s > 0 && s + 1 < NSEG - 1) {
            un0 = lds16(rs, rmap(ubase + ((unsigned)(s + 1) * 32 * 36 + up0 * 72) * 1024, p.sel ^ FARU));
            un1 = lds16(rs, rmap(ubase + ((unsigned)(s + 1) * 32 * 36 + up1 * 72) * 1024, p.sel ^ FARU));
        }
        if (w == 2 && s + 1 + PF < NSEG && bb == 0 && kq < 2) sts16(rs, mem_slot(0, s + 1 + PF) + soff, SENT);
        unsigned cv = 0;
        if (!(s & 1) && s + 2 < NSEG) cv = __builtin_amdgcn_raw_buffer_load_b32(rs, cmap(p, C_U + ((s + 2) >> 1) * 128), 0, 16);
        u32x4 af[9];
#pragma unroll
        for (int k2 = 0; k2 < 9; ++k2) af[k2] = *(const u32x4*)(lds + LB_MEM + k2 * 1024 + l * 16);
        SCHED();
        f32x4 a0 = {0, 0, 0, 0}, a1 = {0, 0, 0, 0};
#pragma unroll
        for (int k2 = 0; k2 < 9; ++k2) {
            a0 = smfmac(af[k2], w0[2 * k2], w0[2 * k2 + 1], a0, idx);
            a1 = smfmac(af[k2], w1[2 * k2], w1[2 * k2 + 1], a1, idx);
        }
        {
            float v[4];
#pragma unroll
            for (int r = 0; r < 4; ++r) {
                auto sw = __builtin_amdgcn_permlane32_swap(__float_as_uint(a0[r]), __float_as_uint(a1[r]), false, false);
                v[r] = __uint_as_float(sw[0]) + __uint_as_float(sw[1]);
            }
            u32x2 o = {pk2(v[0], v[1]), pk2(v[2], v[3])};
            *(u32x2*)(lds + LB_T + toff) = o;
            sts8(rs, rmap(tgo + (unsigned)s * T_SLOT, p.sel ^ TSIDE), (u32x2){nos(o.x), nos(o.y)});
        }
        if (s == NSEG - 1) break;
        if (s == 0) {
            cnt_wait(p, C_U, 64);
            uf0 = lds16(rs, rmap(ubase + (up0 * 72) * 1024, p.sel ^ FARU)); uf1 = lds16(rs, rmap(ubase + (up1 * 72) * 1024, p.sel ^ FARU));
            un0 = lds16(rs, rmap(ubase + (32 * 36 + up0 * 72) * 1024, p.sel ^ FARU)); un1 = lds16(rs, rmap(ubase + (32 * 36 + up1 * 72) * 1024, p.sel ^ FARU));
        }
        bar();
        const char* ps = p.ws + p_slot(s);
        const unsigned sp = (s >> 5) ? selP1 : selP0;
        const unsigned mdst = mem_slot(0, s + 1);
        f32x4 acc = {0, 0, 0, 0};
        dly_wait(tb1, DB);
        int npp;
        if (w < 4) {
            u32x4 tf = *(const u32x4*)(lds + LB_T + ((8 * w + 4 * bb + kq) * 8 + row) * 16);
            const char* aa[3] = {ps + amap(lo_0, sp), ps + amap(lo_1, sp), ps + amap(lo_m, sp)};
            u32x4 v[3];
            polln<3>(aa, v, npp);
            acc = mfma(uf0, v[0], acc);
            acc = mfma(uf1, v[1], acc);
            acc = mfma(tf, v[2], acc);
        } else {
            const char* aa[2] = {ps + amap(lo_0, sp), ps + amap(lo_1, sp)};
            u32x4 v[2];
            polln<2>(aa, v, npp);
            acc = mfma(uf0, v[0], acc);
            acc = mfma(uf1, v[1], acc);
        }
        if (bb == (l >> 5)) {
#pragma unroll
            for (int r = 0; r < 4; ++r) *(float*)(lds + pwo + r * 640) = acc[r];
        }
        bar();
        tb3 = __builtin_amdgcn_s_memtime();
        if (w == 0) {
            const char* pr = lds + LB_PART + ((l & 7) * 8 + (l >> 3)) * 80;
            f32x4 t = (*(const f32x4*)pr + *(const f32x4*)(pr + 16)) + (*(const f32x4*)(pr + 32) + *(const f32x4*)(pr + 48));
            const float x = (t[0] + t[1]) + (t[2] + t[3]);
            const float y = __uint_as_float(__builtin_amdgcn_update_dpp(0u, __float_as_uint(x), 0x101  , 0xf, 0xf, false));
            const unsigned d0 = nos(pk2(x, y));
            const unsigned d1 = __builtin_amdgcn_update_dpp(0u, d0, 0x102, 0xf, 0xf, false);
            const unsigned d2 = __builtin_amdgcn_update_dpp(0u, d0, 0x104, 0xf, 0xf, false);
            const unsigned d3 = __builtin_amdgcn_update_dpp(0u, d0, 0x106, 0xf, 0xf, false);
            if ((l & 7) == 0) { sts16(rs, mdst + sofA, (u32x4){d0, d1, d2, d3}); sts16(rs, mdst + sofB, (u32x4){d0, d1, d2, d3}); }
        }
        if (s > 0) DB = dly_upd(DB, npp, 0, BHI);
        uf0 = un0; uf1 = un1;
        if (!(s & 1) && s + 2 < NSEG && (unsigned)__builtin_amdgcn_readfirstlane(cv) < 32u) cnt_wait(p, C_U + ((s + 2) >> 1) * 128, 32);
    }
}


template <int NC, bool LAST>
DI void task_post(const Ctx& p, rsrc_t rs, char* lds, int s, int ct0) {
    const int w = __builtin_amdgcn_readfirstlane(threadIdx.x >> 6), l = tid_() & 63;
    const int n16 = l & 15, kq = l >> 4;
    f32x4 aa[NC][2];
#pragma unroll
    for (int i = 0; i < NC; ++i) { aa[i][0] = aa[i][1] = (f32x4){0, 0, 0, 0}; }
    const unsigned pt = OFF_PT + (unsigned)s * 32 * 2560;
    const unsigned uf = (unsigned)s * 32 * 36 * 1024;
    cnt_wait(p, C_U + (s >> 1) * 128, s < 2 ? 64 : 32);
    u32x4 yu[4][NC];
#pragma unroll
    for (int hh = 0; hh < 4; ++hh)
#pragma unroll
        for (int i = 0; i < NC; ++i) { const int h = 4 * w + hh; yu[hh][i] = lds16(rs, rmap(uf + ((unsigned)h * 36 + ct0 + i) * 1024, p.sel ^ FARU) + l * 16); }
    const int t = tid_(), tok = t & 31, q = t >> 5, ct = q >> 2, m = q & 3;
    cnt_wait(p, C_BB + (s >> 1) * 128, 3);
    u32x2 bv = {0, 0};
    if (ct < NC) bv = __builtin_amdgcn_raw_buffer_load_b64(rs, OFF_BB + (unsigned)(32 * s + tok) * 1152 + (16 * (ct0 + ct) + 4 * m) * 2, 0, 16);
    const float g = ((const float*)(p.ws + OFF_G))[32 * s + tok];
    cnt_wait(p, C_P + (LAST ? s - NTAIL : pgrp(s)) * 128, 32);
    {
        u32x4 pb[5][2], y[NC];
        for (int spins = 0;; ++spins) {
            asm volatile("" ::: "memory");
#pragma unroll
            for (int i = 0; i < NC; ++i) y[i] = lds16(rs, rmap(OFF_TF + (unsigned)s * T_SLOT + ((unsigned)(ct0 + i) * 32 + 4 * w + kq) * 256 + n16 * 16, p.sel ^ TSIDE));
#pragma unroll
            for (int tt = 0; tt < 2; ++tt) pb[4][tt] = lds16(rs, rmap(pt + (4 * w + kq) * 2560 + (16 * tt + n16) * 16, p.sel ^ PSIDE));
#pragma unroll
            for (int hh = 0; hh < 4; ++hh)
#pragma unroll
                for (int tt = 0; tt < 2; ++tt) pb[hh][tt] = lds16(rs, rmap(pt + (4 * w + hh) * 2560 + (1 + kq) * 512 + (16 * tt + n16) * 16, p.sel ^ PSIDE));
            if (!LAST) break;
            u32x4 m = y[0];
#pragma unroll
            for (int i = 1; i < NC; ++i) m = umax4(m, y[i]);
#pragma unroll
            for (int hh = 0; hh < 5; ++hh) m = umax4(m, umax4(pb[hh][0], pb[hh][1]));
            if (wave_ok(m) || spins > SPIN_CAP) break;
        }
#pragma unroll
        for (int hh = 0; hh < 4; ++hh)
#pragma unroll
            for (int i = 0; i < NC; ++i)
#pragma unroll
                for (int tt = 0; tt < 2; ++tt) aa[i][tt] = mfma(yu[hh][i], pb[hh][tt], aa[i][tt]);
#pragma unroll
        for (int i = 0; i < NC; ++i) {
#pragma unroll
            for (int tt = 0; tt < 2; ++tt) aa[i][tt] = mfma(y[i], pb[4][tt], aa[i][tt]);
        }
    }
#pragma unroll
    for (int i = 0; i < NC; ++i)
#pragma unroll
        for (int tt = 0; tt < 2; ++tt) *(f32x4*)(lds + ((w * NC + i) * 2 + tt) * 1024 + l * 16) = aa[i][tt];
    bar();
    if (ct < NC) {
        const int tt = tok >> 4, t16 = tok & 15;
        const int k0 = m >> 1, r0 = 2 * (m & 1);
        f32x2 a01 = {0, 0}, a23 = {0, 0};
#pragma unroll
        for (int ww = 0; ww < 8; ++ww) {
            const char* ba = lds + ((ww * NC + ct) * 2 + tt) * 1024;
            a01 += *(const f32x2*)(ba + (t16 + 16 * k0) * 16 + r0 * 4);
            a23 += *(const f32x2*)(ba + (t16 + 16 * (k0 + 2)) * 16 + r0 * 4);
        }
        const float og = rbf(1.f - g);
        float o0 = rbf(g * rbf(a01[0])) + rbf(og * __uint_as_float(bv.x << 16));
        float o1 = rbf(g * rbf(a01[1])) + rbf(og * __uint_as_float(bv.x & 0xffff0000u));
        float o2 = rbf(g * rbf(a23[0])) + rbf(og * __uint_as_float(bv.y << 16));
        float o3 = rbf(g * rbf(a23[1])) + rbf(og * __uint_as_float(bv.y & 0xffff0000u));
        *(u32x2*)((char*)p.out + (size_t)(32 * s + tok) * 1152 + (16 * (ct0 + ct) + 4 * m) * 2) = (u32x2){pk2(o0, o1), pk2(o2, o3)};
    }
    bar();
}

__global__ void __launch_bounds__(512, 2) k_mega(Ptrs pin) {
    extern __shared__ __attribute__((aligned(16))) char lds[];
    Ctx p;
    p.x = uni(pin.x); p.mem0 = uni(pin.mem0); p.Wq = uni(pin.Wq); p.Wk = uni(pin.Wk); p.Wv = uni(pin.Wv); p.Wo = uni(pin.Wo);
    p.bw = uni(pin.bw); p.bo = uni(pin.bo); p.gw = uni(pin.gw); p.out = uni(pin.out); p.ws = uni(pin.ws);
    p.sel = load_selmask(p.ws) & 1u;
    unsigned* coff = (unsigned*)(p.ws + C_OFF);
    if (blockIdx.x == 0 && threadIdx.x == 0) __hip_atomic_store(coff, xcc_id() + 1u, __ATOMIC_RELAXED, __HIP_MEMORY_SCOPE_AGENT);
    const rsrc_t rs = mk_rsrc(p.ws);
    unsigned off = 0;
    for (int spins = 0; spins < (1 << 20); ++spins) { off = __hip_atomic_load(coff, __ATOMIC_RELAXED, __HIP_MEMORY_SCOPE_AGENT); if (off) break; }
    off = __builtin_amdgcn_readfirstlane(off) - 1u;
    const int g = (blockIdx.x + off) & 7, i = blockIdx.x >> 3;
    if (g == 0) { role_A(p, rs, lds, i); }
    else {
        const bool isw = g >= 4;
        const int h = 8 * (g - 4) + (i >> 2), sub = i & 3;
        const int widx = i * 4 + (g - 4);
        int bg = -1;
        if (!isw) {
            const int k = (g - 1) * 32 + i;
            fold_unit<12, 18 - WFK>(p, rs, lds, k / 3, (k % 3) * 12, WFK, C_F2 + (k % 3) * 128);
            if (i < 24) role_B(p, rs, lds, (g - 1) * 24 + i);
            else if ((i - 24) * 3 + (g - 1) < 8) bg = (i - 24) * 3 + (g - 1);
        } else {
            {
                const int w = __builtin_amdgcn_readfirstlane(threadIdx.x >> 6), l = tid_() & 63;
                if (sub < 2) qk_issue(p, lds_addr(lds), h, w, l, 0); else vu_issue(p, lds_addr(lds), h, w, l, 0);
            }
            fold_unit<9, WFK>(p, rs, lds, h, sub * 9, 0, 0u);
            if (sub < 2) worker_qk(p, rs, lds, h, sub, C_F + sub * 128); else worker_vu(p, rs, lds, h, sub & 1, C_F + sub * 128);
            if (sub < 2) bg = 8 + (i >> 2) * 8 + sub * 4 + (g - 4);
        }
        if (bg >= 0) bgemm_task(p, rs, lds, bg);
        if (isw) {
            for (int t = widx; t < 256; t += 128) task_battn(p, rs, lds, t >> 5, t & 31);
            if (widx < 96) task_bout(p, rs, lds, widx % 3, widx / 3);
            for (int t = (widx + 32) & 127; t < (NSEG - NTAIL) * 9; t += 128) task_post<4, false>(p, rs, lds, t / 9, (t % 9) * 4);
            if (widx >= 20 && widx < 56) task_post<1, true>(p, rs, lds, NSEG - 1, widx - 20);
            else if (widx >= 56 && widx < 92) task_post<2, true>(p, rs, lds, NSEG - 3 + (widx - 56) / 18, ((widx - 56) % 18) * 2);
        }
    }
}

extern "C" void kernel_launch(void* const* d_in, const int* in_sizes, int n_in,
                              void* d_out, int out_size, void* d_ws, size_t ws_size,
                              hipStream_t stream) {
    Ptrs p;
    p.x = (const u16*)d_in[0]; p.mem0 = (const u16*)d_in[1];
    p.Wq = (const u16*)d_in[2]; p.Wk = (const u16*)d_in[3]; p.Wv = (const u16*)d_in[4]; p.Wo = (const u16*)d_in[5];
    p.bw = (const u16*)d_in[8]; p.bo = (const u16*)d_in[9]; p.gw = (const u16*)d_in[10];
    p.out = (u16*)d_out;
    p.ws = (char*)(((uintptr_t)d_ws + MB2 - 1) & ~(uintptr_t)(MB2 - 1));
    (void)hipFuncSetAttribute((const void*)k_mega, hipFuncAttributeMaxDynamicSharedMemorySize, XL_BYTES);
    k_init<<<NB_INIT + 1, 256, 0, stream>>>(p);
    k_mega<<<256, 512, XL_BYTES, stream>>>(p);
}
```
